```python
import torch

def _softmax(x, axis=-1):
    return torch.softmax(x, dim=axis)
def _gelu(x):
    return torch.nn.functional.gelu(x, approximate="tanh")
def _silu(x):
    return torch.nn.functional.silu(x)
def _sigmoid(x):
    return torch.sigmoid(x)
def _layernorm(x, axis=-1, eps=1e-5):
    m = x.mean(dim=axis, keepdim=True)
    v = x.var(dim=axis, keepdim=True, unbiased=False)
    return (x - m) / torch.sqrt(v + eps)
def _scatter_add(tgt, idx, src, axis=0):
    return tgt.clone().index_add_(axis, idx, src)
def _segment_sum(src, seg, n):
    out = torch.zeros((n,) + src.shape[1:], dtype=src.dtype)
    return out.index_add_(0, seg, src)
def _pad(x, pw):
    flat = tuple(v for pair in reversed(pw) for v in pair)
    return torch.nn.functional.pad(x, flat)

kernel_name = 'gated_mix__sliding_block__geglu_ffn__prompt'

def setup_inputs():

    _w = (lambda a: (lambda t: t.to(torch.bfloat16) if t.is_floating_point() else t)(torch.as_tensor(a)))
    def _A_setup():
        B, S, D = 1, 2048, 1024
        return {
            "x": _w((lambda *s: torch.randn(*s, dtype=torch.bfloat16) * 0.05)(B, S, D)),
            "wqkv": _w((lambda *s: torch.randn(*s, dtype=torch.bfloat16) * 1.6)(D, 3 * D)),
            "wo": _w((lambda *s: torch.randn(*s, dtype=torch.bfloat16) * 0.1)(D, D)),
        }
    _A_in = _A_setup()

    def _B_setup():
        B, S, D, F = 1, 2048, 1024, 3584
        return {
            "x": _w((lambda *s: torch.randn(*s, dtype=torch.bfloat16) * 0.05)(B, S, D)),
            "w_gate": _w((lambda *s: torch.randn(*s, dtype=torch.bfloat16) * 0.4)(D, F)),
            "w_up": _w((lambda *s: torch.randn(*s, dtype=torch.bfloat16) * 0.4)(D, F)),
            "w_down": _w((lambda *s: torch.randn(*s, dtype=torch.bfloat16) * 0.4)(F, D)),
        }
    _B_in = _B_setup()

    return {
        "A_x": _A_in["x"],
        "A_wqkv": _A_in["wqkv"],
        "A_wo": _A_in["wo"],
        "B_x": _B_in["x"],
        "B_w_gate": _B_in["w_gate"],
        "B_w_up": _B_in["w_up"],
        "B_w_down": _B_in["w_down"],
        "gate_w": _w((lambda *s: torch.randn(*s, dtype=torch.bfloat16) * 0.05)(1024) * 16.0),
    }

def reference(A_x, A_wqkv, A_wo, B_x, B_w_gate, B_w_up, B_w_down, gate_w):
    def _A_ref(x, wqkv, wo):
        B, S, D = x.shape
        H = 32; Dh = D // H; W = 256
        qkv = (lambda x, *d: x.permute(*d))((x @ wqkv).reshape(B, S, 3, H, Dh), 2, 0, 3, 1, 4)
        q, k, v = qkv[0], qkv[1], qkv[2]
        scores = torch.einsum("bhqd,bhkd->bhqk", q, k) * (Dh ** -0.5)
        idx = torch.arange(S)
        mask = (idx[:, None] - idx[None, :] >= 0) & (idx[:, None] - idx[None, :] < W)
        scores = torch.where(mask[None, None], scores, -1e9)
        p = _softmax(scores, axis=-1)
        o = torch.einsum("bhqk,bhkd->bhqd", p, v)
        return (lambda x, *d: x.permute(*d))(o, 0, 2, 1, 3).reshape(B, S, D) @ wo

    def _B_ref(x, w_gate, w_up, w_down):
        return (_gelu(x @ w_gate) * (x @ w_up)) @ w_down

    x = A_x
    g = _sigmoid((x * gate_w).sum(-1, keepdims=True))
    a = _A_ref(x, A_wqkv, A_wo)
    b = _B_ref(x, B_w_gate, B_w_up, B_w_down)
    return g * a + (1 - g) * b
```

```cpp
#include <hip/hip_runtime.h>
#include <hip/hip_bf16.h>
#include <stdint.h>

#define LAS __attribute__((address_space(3)))
typedef unsigned short u16;
typedef short bf16x8 __attribute__((ext_vector_type(8)));
typedef short s16x4 __attribute__((ext_vector_type(4)));
typedef float f32x4 __attribute__((ext_vector_type(4)));
typedef float f32x16 __attribute__((ext_vector_type(16)));
typedef unsigned u32x4 __attribute__((ext_vector_type(4)));
typedef unsigned u32x2 __attribute__((ext_vector_type(2)));

constexpr int S_ = 2048, D_ = 1024, F_ = 3584, NQKV = 3072, K2 = F_ + D_;
constexpr float QSCALE = 0.17677669529663687f * 1.4426950408889634f;

constexpr size_t WS_QKV = 0;
constexpr size_t WS_A2 = WS_QKV + (size_t)NQKV * S_ * 2;
constexpr size_t WS_G = WS_A2 + (size_t)K2 * S_ * 2;
constexpr size_t WS_CNT = WS_G + S_ * 4;
constexpr size_t WS_PART = WS_CNT + 64 * 256;

#define WAIT_V(n) asm volatile("s_waitcnt vmcnt(" #n ")" ::: "memory")
#define WAIT_L0 asm volatile("s_waitcnt lgkmcnt(0)" ::: "memory")
#define BAR __builtin_amdgcn_s_barrier()
#define SCHED __builtin_amdgcn_sched_barrier(0)

__device__ __forceinline__ void glds16(const void* sbase, unsigned voff, unsigned lds_addr) {
  asm volatile("s_mov_b32 m0, %2\n\ts_nop 0\n\tglobal_load_lds_dwordx4 %0, %1" ::"v"(voff), "s"(sbase), "s"(lds_addr) : "memory");
}
__device__ __forceinline__ unsigned cvtpk(float lo, float hi) {
  unsigned r;
  asm("v_cvt_pk_bf16_f32 %0, %1, %2" : "=v"(r) : "v"(lo), "v"(hi));
  return r;
}
__device__ __forceinline__ bf16x8 lds_b128(const char LAS* p) { return *(const LAS bf16x8*)p; }
__device__ __forceinline__ s16x4 lds_tr(const char LAS* p) {
  return __builtin_bit_cast(s16x4, __builtin_amdgcn_ds_read_tr16_b64_v4i16((LAS s16x4*)p));
}
__device__ __forceinline__ bf16x8 cat8(s16x4 a, s16x4 b) {
  return (bf16x8){a[0], a[1], a[2], a[3], b[0], b[1], b[2], b[3]};
}
__device__ __forceinline__ float bf2f(unsigned short v) { return __uint_as_float((unsigned)v << 16); }
__device__ __forceinline__ float gelu_t(float x) {
  const float c0 = -2.0f * 0.7978845608028654f * 1.4426950408889634f;
  const float c1 = c0 * 0.044715f;
  float t = x * x;
  float z = x * __builtin_fmaf(t, c1, c0);
  float e = __builtin_amdgcn_exp2f(z);
  return x * __builtin_amdgcn_rcpf(1.0f + e);
}

constexpr int G1_LDS_A = 0;
constexpr int G1_LDS_B = 65536;
constexpr int G1_LDS_B4 = 131072;
constexpr int G1_LDS = 147456;

__device__ __forceinline__ void store_pair16(u16* dst_blk  , int row0, int lane, unsigned x0,
                                             unsigned x1, unsigned y0, unsigned y1) {
  auto r0 = __builtin_amdgcn_permlane16_swap(x0, y0, false, false);
  auto r1 = __builtin_amdgcn_permlane16_swap(x1, y1, false, false);
  const int q = lane >> 4;
  const int row = row0 + (q & 1) * 16 + (lane & 15);
  u32x4 w = {r0[0], r1[0], r0[1], r1[1]};
  *(u32x4*)(dst_blk + (size_t)row * 16 + (q >> 1) * 8) = w;
  SCHED;
}

__global__ __launch_bounds__(512, 2) void k_gemm1(const u16* __restrict__ x, const u16* __restrict__ wqkv, const u16* __restrict__ wg,
                                                  const u16* __restrict__ wu, const u16* __restrict__ gate_w, char* __restrict__ ws) {
  extern __shared__ __attribute__((aligned(16))) char lds_[];
  char LAS* lds = (char LAS*)lds_;
  const unsigned lds0 = (unsigned)(uintptr_t)lds;
  const int tid = threadIdx.x, lane = tid & 63;
  const int wid = __builtin_amdgcn_readfirstlane(tid >> 6);
  const int wr = wid >> 2, wc = wid & 3;
  const int bid = blockIdx.x;
  const int xcd = bid & 7, idx = bid >> 3;
  const int tn = xcd * 4 + (idx & 3), tm = idx >> 2;

  int ptype[5], pcol[5];
  if (tn < 9) {
#pragma unroll
    for (int p = 0; p < 5; ++p) { ptype[p] = 0; pcol[p] = (tn * 5 + p) * 64; }
  } else if (tn == 9) {
    ptype[0] = 1; pcol[0] = 0; ptype[1] = 2; pcol[1] = 0;
    ptype[2] = 0; pcol[2] = 45 * 64; ptype[3] = 0; pcol[3] = 46 * 64; ptype[4] = 0; pcol[4] = 47 * 64;
  } else {
    const int t = tn - 10, F0 = 64 + 160 * t, odd = t & 1;
    const int g0 = F0 + (odd ? 32 : 0), g1 = g0 + 64, fm = odd ? F0 : F0 + 128;
    ptype[0] = 1; pcol[0] = g0; ptype[1] = 2; pcol[1] = g0;
    ptype[2] = 1; pcol[2] = g1; ptype[3] = 2; pcol[3] = g1;
    ptype[4] = 3; pcol[4] = fm;
  }

  const char* sA = (const char*)x + (size_t)(tm * 256 + wid * 8) * 2048;
  unsigned voffA;
  { const int r = lane >> 3, j = lane & 7; voffA = r * 2048 + ((j ^ ((4 * (wid & 1) + (r >> 1)) & 7)) << 4); }
  const unsigned ldsA = lds0 + G1_LDS_A + wid * 1024;
  const int myp = wid >> 1;
  int mt = ptype[0], mc = pcol[0];
#pragma unroll
  for (int p = 1; p < 4; ++p) if (myp == p) { mt = ptype[p]; mc = pcol[p]; }
  const unsigned strB = (mt == 0) ? 6144u : 7168u;
  const char* sB = (const char*)(mt == 0 ? wqkv : (mt == 1 ? wg : wu)) + (size_t)mc * 2 + (size_t)(16 * (wid & 1)) * strB;
  unsigned voffB0, voffB1;
  { const int r = lane >> 3, j = lane & 7, q = r & 3;
    const int f0 = (q >> 1) & 1, f1 = f0 | 2;
    voffB0 = r * strB + ((((j >> 1) ^ f0) << 1 | (j & 1)) << 4);
    voffB1 = (r + 8) * strB + ((((j >> 1) ^ f1) << 1 | (j & 1)) << 4); }
  const unsigned ldsB = lds0 + G1_LDS_B + myp * 8192 + (wid & 1) * 2048;
  const char* sB4; unsigned voffB4, strB4;
  if (ptype[4] == 0) {
    strB4 = 6144u;
    sB4 = (const char*)wqkv + (size_t)pcol[4] * 2 + (size_t)(8 * wid) * strB4;
    const int r = lane >> 3, j = lane & 7, q = r & 3;
    const int f = ((q >> 1) & 1) | ((wid & 1) << 1);
    voffB4 = r * strB4 + ((((j >> 1) ^ f) << 1 | (j & 1)) << 4);
  } else {
    strB4 = 7168u;
    const int isUp = (wid >> 1) & 1;
    sB4 = (const char*)(isUp ? wu : wg) + (size_t)pcol[4] * 2 + (size_t)(32 * (wid >> 2) + 16 * (wid & 1)) * strB4;
    const int t = lane >> 5, glo = (lane >> 4) & 1, q = (lane >> 2) & 3, gr = lane & 3;
    voffB4 = (8 * glo + 4 * t + q) * strB4 + ((gr ^ (glo | (isUp << 1))) << 4);
  }
  const unsigned ldsB4 = lds0 + G1_LDS_B4 + wid * 1024;

#define G1_A0(kt, b) do { glds16(sA + (size_t)(kt) * 128, voffA, ldsA + (b) * 32768); \
                          glds16(sA + (size_t)(kt) * 128 + 64 * 2048, voffA, ldsA + (b) * 32768 + 8192); } while (0)
#define G1_A1(kt, b) do { glds16(sA + (size_t)(kt) * 128 + 128 * 2048, voffA, ldsA + (b) * 32768 + 16384); \
                          glds16(sA + (size_t)(kt) * 128 + 192 * 2048, voffA, ldsA + (b) * 32768 + 24576); } while (0)
#define G1_X(kt, b) do { const char* _p = sB + (size_t)(kt) * 64 * strB; glds16(_p, voffB0, ldsB + (b) * 32768); \
                         glds16(_p, voffB1, ldsB + (b) * 32768 + 1024); } while (0)
#define G1_Y(kt, b) do { const char* _p = sB + (size_t)((kt) * 64 + 32) * strB; glds16(_p, voffB0, ldsB + (b) * 32768 + 4096); \
                         glds16(_p, voffB1, ldsB + (b) * 32768 + 4096 + 1024); } while (0)
#define G1_Z(kt, b) glds16(sB4 + (size_t)(kt) * 64 * strB4, voffB4, ldsB4 + (b) * 8192)

  G1_X(0, 0); G1_Z(0, 0); G1_A0(0, 0); G1_A1(0, 0); G1_Y(0, 0); G1_X(1, 1); G1_Z(1, 1);

  const int fr = lane & 15, g4 = lane >> 4;
  const char LAS* rA0 = lds + G1_LDS_A + wr * 8192 + fr * 128 + (((0 + g4) ^ (fr >> 1)) << 4);
  const char LAS* rA1 = lds + G1_LDS_A + wr * 8192 + fr * 128 + (((4 + g4) ^ (fr >> 1)) << 4);
  const char LAS* rB; const char LAS* rB4;
  { const int q = (lane & 15) >> 2, p = lane & 3;
    const int f = ((q >> 1) & 1) | ((g4 & 1) << 1);
    const int o = g4 * 1024 + q * 128 + ((wc ^ f) << 5) + p * 8;
    rB = lds + G1_LDS_B + o;
    const int isUp = p >> 1;
    const int om = (isUp << 11) | ((g4 >> 1) << 10) | ((g4 & 1) << 8) | (q << 6) | ((wc ^ ((g4 & 1) | (isUp << 1))) << 4) | ((p & 1) << 3);
    rB4 = lds + G1_LDS_B4 + (ptype[4] == 0 ? o : om); }

  f32x4 acc[2][4][5];
#pragma unroll
  for (int a = 0; a < 2; ++a)
#pragma unroll
    for (int m = 0; m < 4; ++m)
#pragma unroll
      for (int p = 0; p < 5; ++p) acc[a][m][p] = (f32x4){0.f, 0.f, 0.f, 0.f};
  bf16x8 Af[4], Bf[5];

#define G1_LDB(b, ks) do { _Pragma("unroll") for (int p = 0; p < 4; ++p) { \
      s16x4 lo = lds_tr(rB + (b) * 32768 + p * 8192 + (ks) * 4096), hi = lds_tr(rB + (b) * 32768 + p * 8192 + (ks) * 4096 + 512); Bf[p] = cat8(lo, hi); } \
    { s16x4 lo = lds_tr(rB4 + (b) * 8192 + (ks) * 4096), hi = lds_tr(rB4 + (b) * 8192 + (ks) * 4096 + 512); Bf[4] = cat8(lo, hi); } } while (0)
#define G1_LDA(b, ai, ks) do { _Pragma("unroll") for (int m = 0; m < 4; ++m) \
      Af[m] = lds_b128(((ks) ? rA1 : rA0) + (b) * 32768 + (ai) * 16384 + m * 2048); } while (0)
#define G1_MMA(ai) do { __builtin_amdgcn_s_setprio(1); _Pragma("unroll") for (int m = 0; m < 4; ++m) _Pragma("unroll") for (int p = 0; p < 5; ++p) \
      acc[ai][m][p] = __builtin_amdgcn_mfma_f32_16x16x32_bf16(Bf[p], Af[m], acc[ai][m][p], 0, 0, 0); __builtin_amdgcn_s_setprio(0); } while (0)

  WAIT_V(7); BAR;
  if (wr == 1) BAR;

#define G1_TILE(t, b, I1, I2, W0, W1, W3) do { \
    G1_LDB(b, 0); G1_LDA(b, 0, 0); if (I1) G1_A0((t) + 1, (b) ^ 1); W0; BAR; WAIT_L0; G1_MMA(0); BAR; SCHED; \
    G1_LDA(b, 1, 0); if (I1) { G1_A1((t) + 1, (b) ^ 1); G1_Y((t) + 1, (b) ^ 1); } W1; BAR; WAIT_L0; G1_MMA(1); BAR; SCHED; \
    G1_LDB(b, 1); G1_LDA(b, 0, 1); BAR; WAIT_L0; G1_MMA(0); BAR; SCHED; \
    G1_LDA(b, 1, 1); if (I2) { G1_X((t) + 2, b); G1_Z((t) + 2, b); } W3; BAR; WAIT_L0; G1_MMA(1); BAR; SCHED; \
  } while (0)

  for (int t = 0; t < 14; t += 2) {
    G1_TILE(t, 0, 1, 1, WAIT_V(7), WAIT_V(9), WAIT_V(7));
    G1_TILE(t + 1, 1, 1, 1, WAIT_V(7), WAIT_V(9), WAIT_V(7));
  }
  G1_TILE(14, 0, 1, 0, WAIT_V(7), WAIT_V(9), WAIT_V(4));
  G1_TILE(15, 1, 0, 0, WAIT_V(2), WAIT_V(0), (void)0);
  if (wr == 0) BAR;
  int lane_e = __builtin_amdgcn_mbcnt_hi(~0u, __builtin_amdgcn_mbcnt_lo(~0u, 0u));
  asm volatile("" : "+v"(lane_e));
#define lane lane_e

  {
    const int row = bid * 8 + wid;
    const u32x4* xr = (const u32x4*)(x + (size_t)row * D_) + lane * 2;
    const u32x4* gw = (const u32x4*)gate_w + lane * 2;
    u32x4 a0 = xr[0], a1 = xr[1], b0 = gw[0], b1 = gw[1];
    float s = 0.f;
#pragma unroll
    for (int i = 0; i < 4; ++i) {
      s += __uint_as_float(a0[i] << 16) * __uint_as_float(b0[i] << 16) + __uint_as_float(a0[i] & 0xffff0000u) * __uint_as_float(b0[i] & 0xffff0000u);
      s += __uint_as_float(a1[i] << 16) * __uint_as_float(b1[i] << 16) + __uint_as_float(a1[i] & 0xffff0000u) * __uint_as_float(b1[i] & 0xffff0000u);
    }
#pragma unroll
    for (int o = 32; o >= 1; o >>= 1) s += __shfl_xor(s, o);
    if (lane == 0) ((float*)(ws + WS_G))[row] = 1.0f / (1.0f + __expf(-s));
    if (bid == 0 && wid == 0) ((unsigned*)(ws + WS_CNT))[lane * 64] = 0u;
  }

  u16* qkv = (u16*)(ws + WS_QKV);
  u16* a2 = (u16*)(ws + WS_A2);
  const int rowb = tm * 256 + wr * 64;
#pragma unroll
  for (int pp = 0; pp < 2; ++pp) {
    if (ptype[2 * pp] == 0) {
#pragma unroll
      for (int h = 0; h < 2; ++h) {
        const int p = 2 * pp + h;
        const float sc = pcol[p] < D_ ? QSCALE : 1.0f;
        u16* dst = qkv + (size_t)(pcol[p] / 16 + wc) * S_ * 16;
#pragma unroll
        for (int a = 0; a < 2; ++a)
#pragma unroll
          for (int m = 0; m < 4; m += 2) {
            const f32x4 u = acc[a][m][p] * sc, v = acc[a][m + 1][p] * sc;
            store_pair16(dst, rowb + a * 128 + m * 16, lane, cvtpk(u[0], u[1]), cvtpk(u[2], u[3]), cvtpk(v[0], v[1]), cvtpk(v[2], v[3]));
          }
      }
    } else {
      u16* dst = a2 + (size_t)(pcol[2 * pp] / 16 + wc) * S_ * 16;
#pragma unroll
      for (int a = 0; a < 2; ++a)
#pragma unroll
        for (int m = 0; m < 4; m += 2) {
          f32x4 u, v;
#pragma unroll
          for (int j = 0; j < 4; ++j) {
            u[j] = gelu_t(acc[a][m][2 * pp][j]) * acc[a][m][2 * pp + 1][j];
            v[j] = gelu_t(acc[a][m + 1][2 * pp][j]) * acc[a][m + 1][2 * pp + 1][j];
          }
          store_pair16(dst, rowb + a * 128 + m * 16, lane, cvtpk(u[0], u[1]), cvtpk(u[2], u[3]), cvtpk(v[0], v[1]), cvtpk(v[2], v[3]));
        }
    }
  }
  if (ptype[4] == 0) {
    const float sc = pcol[4] < D_ ? QSCALE : 1.0f;
    u16* dst = qkv + (size_t)(pcol[4] / 16 + wc) * S_ * 16;
#pragma unroll
    for (int a = 0; a < 2; ++a)
#pragma unroll
      for (int m = 0; m < 4; m += 2) {
        const f32x4 u = acc[a][m][4] * sc, v = acc[a][m + 1][4] * sc;
        store_pair16(dst, rowb + a * 128 + m * 16, lane, cvtpk(u[0], u[1]), cvtpk(u[2], u[3]), cvtpk(v[0], v[1]), cvtpk(v[2], v[3]));
      }
  } else {
    u16* dst = a2 + (size_t)((pcol[4] + 8 * wc) / 16) * S_ * 16 + (wc & 1) * 8;
#pragma unroll
    for (int a = 0; a < 2; ++a) {
      unsigned pk[2][2];
#pragma unroll
      for (int mp = 0; mp < 2; ++mp) {
        float hv[4];
#pragma unroll
        for (int j = 0; j < 4; ++j) {
          auto r = __builtin_amdgcn_permlane32_swap(__float_as_uint(acc[a][2 * mp][4][j]), __float_as_uint(acc[a][2 * mp + 1][4][j]), false, false);
          hv[j] = gelu_t(__uint_as_float(r[0])) * __uint_as_float(r[1]);
        }
        pk[mp][0] = cvtpk(hv[0], hv[1]); pk[mp][1] = cvtpk(hv[2], hv[3]);
      }
      auto r0 = __builtin_amdgcn_permlane16_swap(pk[0][0], pk[1][0], false, false);
      auto r1 = __builtin_amdgcn_permlane16_swap(pk[0][1], pk[1][1], false, false);
      const int q = lane >> 4;
      const int row = rowb + a * 128 + ((q & 1) * 2 + (q >> 1)) * 16 + (lane & 15);
      u32x4 w = {r0[0], r1[0], r0[1], r1[1]};
      *(u32x4*)(dst + (size_t)row * 16) = w;
      SCHED;
    }
  }
#undef lane
}

constexpr int AT_K = 0;
constexpr int AT_V = 32768;
constexpr int AT_V1 = AT_V + 16384 + 128;
constexpr int AT_LDS = AT_V1 + 16384;

__global__ __launch_bounds__(512, 2) void k_attn(char* __restrict__ ws) {
  extern __shared__ __attribute__((aligned(16))) char lds_[];
  char LAS* lds = (char LAS*)lds_;
  const unsigned lds0 = (unsigned)(uintptr_t)lds;
  const int tid = threadIdx.x, lane = tid & 63;
  const int wid = __builtin_amdgcn_readfirstlane(tid >> 6);
  const int bid = blockIdx.x;
  const int head = (bid & 7) * 4 + ((bid >> 3) & 3), rb = bid >> 5;
  const int q0 = rb * 256;
  const char* qkv = ws + WS_QKV;
  {
    const unsigned voffK = (lane >> 1) * 32 + (((lane & 1) ^ ((lane >> 4) & 1)) << 4);
    const unsigned voffV = lane * 16;
#pragma unroll
    for (int i = 0; i < 2; ++i) {
      const int rp = wid + 8 * i;
      if (rb > 0 || rp >= 8) {
        const int row = q0 - 256 + rp * 32;
#pragma unroll
        for (int dh = 0; dh < 2; ++dh) {
          const char* kp = qkv + ((size_t)(64 + head * 2 + dh) * S_ + row) * 32;
          const char* vp = qkv + ((size_t)(128 + head * 2 + dh) * S_ + row) * 32;
          glds16(kp, voffK, lds0 + AT_K + dh * 16384 + rp * 1024);
          glds16(vp, voffV, lds0 + (dh ? AT_V1 : AT_V) + rp * 1024);
        }
      }
    }
  }
  const int r32 = lane & 31, hi = lane >> 5;
  const int qrow = q0 + wid * 32 + r32;
  bf16x8 qf[2];
#pragma unroll
  for (int dh = 0; dh < 2; ++dh) qf[dh] = *(const bf16x8*)(qkv + ((size_t)(head * 2 + dh) * S_ + qrow) * 32 + hi * 16);
  const float gval = ((const float*)(ws + WS_G))[qrow];
  WAIT_V(0);
  __syncthreads();

  const char LAS* kbase = lds + AT_K + wid * 1024 + r32 * 32 + ((hi ^ ((r32 >> 3) & 1)) << 4);
  const int kb0 = (rb == 0) ? (8 - wid) : 0;
  f32x16 s[9];
#pragma unroll
  for (int kb = 0; kb < 9; ++kb) {
    f32x16 z = {0.f, 0.f, 0.f, 0.f, 0.f, 0.f, 0.f, 0.f, 0.f, 0.f, 0.f, 0.f, 0.f, 0.f, 0.f, 0.f};
    if (kb >= kb0) {
      bf16x8 k0 = lds_b128(kbase + kb * 1024), k1 = lds_b128(kbase + 16384 + kb * 1024);
      z = __builtin_amdgcn_mfma_f32_32x32x16_bf16(k0, qf[0], z, 0, 0, 0);
      z = __builtin_amdgcn_mfma_f32_32x32x16_bf16(k1, qf[1], z, 0, 0, 0);
    } else {
#pragma unroll
      for (int r = 0; r < 16; ++r) z[r] = -INFINITY;
    }
    s[kb] = z;
  }
#pragma unroll
  for (int r = 0; r < 16; ++r) {
    const int c = (r & 3) + 8 * (r >> 2) + 4 * hi;
    if (c <= r32) s[0][r] = -INFINITY;
    if (c > r32) s[8][r] = -INFINITY;
  }
  float m = s[8][0];
#pragma unroll
  for (int kb = 0; kb < 9; ++kb)
#pragma unroll
    for (int r = 0; r < 16; ++r) m = fmaxf(m, s[kb][r]);
  { auto rr = __builtin_amdgcn_permlane32_swap(__float_as_uint(m), __float_as_uint(m), false, false);
    m = fmaxf(__uint_as_float(rr[0]), __uint_as_float(rr[1])); }
  float l = 0.f;
  f32x16 o = {0.f, 0.f, 0.f, 0.f, 0.f, 0.f, 0.f, 0.f, 0.f, 0.f, 0.f, 0.f, 0.f, 0.f, 0.f, 0.f};
  const char LAS* vbase;
  { const int g = lane >> 4, q = (lane & 15) >> 2, p = lane & 3;
    vbase = lds + ((g & 1) ? AT_V1 : AT_V) + wid * 1024 + (4 * (g >> 1) + q) * 32 + p * 8; }
#pragma unroll
  for (int kb = 0; kb < 9; ++kb) {
    if (kb >= kb0) {
      unsigned pk[8];
#pragma unroll
      for (int r = 0; r < 16; r += 2) {
        const float e0 = __builtin_amdgcn_exp2f(s[kb][r] - m), e1 = __builtin_amdgcn_exp2f(s[kb][r + 1] - m);
        l += e0 + e1;
        pk[r >> 1] = cvtpk(e0, e1);
      }
#pragma unroll
      for (int st = 0; st < 2; ++st) {
        s16x4 lo = lds_tr(vbase + kb * 1024 + st * 512), hi4 = lds_tr(vbase + kb * 1024 + st * 512 + 256);
        u32x4 pw = {pk[4 * st], pk[4 * st + 1], pk[4 * st + 2], pk[4 * st + 3]};
        o = __builtin_amdgcn_mfma_f32_32x32x16_bf16(cat8(lo, hi4), __builtin_bit_cast(bf16x8, pw), o, 0, 0, 0);
      }
    }
  }
  { auto rr = __builtin_amdgcn_permlane32_swap(__float_as_uint(l), __float_as_uint(l), false, false);
    l = __uint_as_float(rr[0]) + __uint_as_float(rr[1]); }
  const float sc = gval / l;
  u16* a2 = (u16*)(ws + WS_A2);
#pragma unroll
  for (int dh = 0; dh < 2; ++dh) {
    const unsigned a0 = cvtpk(o[8 * dh + 0] * sc, o[8 * dh + 1] * sc), a1 = cvtpk(o[8 * dh + 2] * sc, o[8 * dh + 3] * sc);
    const unsigned b0 = cvtpk(o[8 * dh + 4] * sc, o[8 * dh + 5] * sc), b1 = cvtpk(o[8 * dh + 6] * sc, o[8 * dh + 7] * sc);
    auto r0 = __builtin_amdgcn_permlane32_swap(a0, b0, false, false);
    auto r1 = __builtin_amdgcn_permlane32_swap(a1, b1, false, false);
    u32x4 w = {r0[0], r1[0], r0[1], r1[1]};
    *(u32x4*)(a2 + ((size_t)(224 + head * 2 + dh) * S_ + qrow) * 16 + hi * 8) = w;
  }
}

constexpr int G2_STAGE = 24576;
constexpr int G2_NST = 6;
constexpr int G2_LDS = G2_STAGE * G2_NST;
constexpr int G2_SPLIT_ST = 36;

__global__ __launch_bounds__(512, 2) void k_gemm2(const u16* __restrict__ wd, const u16* __restrict__ wo, char* __restrict__ ws,
                                                  u16* __restrict__ out) {
  extern __shared__ __attribute__((aligned(16))) char lds_[];
  char LAS* lds = (char LAS*)lds_;
  const unsigned lds0 = (unsigned)(uintptr_t)lds;
  const int tid = threadIdx.x, lane = tid & 63;
  const int wid = __builtin_amdgcn_readfirstlane(tid >> 6);
  const int wm = wid & 3, wn = wid >> 2;
  const int bid = blockIdx.x;
  const int xcd = bid & 7, idx = bid >> 3;
  const int sp = idx & 3, tn = (xcd & 1) * 4 + ((idx >> 2) & 3), tm = (xcd >> 1) * 2 + (idx >> 4);
  const int tile = tm * 8 + tn;
  const int st0 = sp * G2_SPLIT_ST;

  const char* sA = ws + WS_A2 + ((size_t)(st0 * 2) * S_ + tm * 256 + wid * 32) * 32;
  const unsigned voffA = lane * 16;
  unsigned voffB;
  { const int r = lane >> 3, j = lane & 7, q = r & 3;
    const int f = ((q >> 1) & 1) | ((wid & 1) << 1);
    voffB = r * 2048 + ((((j >> 1) ^ f) << 1 | (j & 1)) << 4); }
  const size_t boff = (size_t)(8 * (wid & 3)) * 2048 + (size_t)(tn * 128 + (wid >> 2) * 64) * 2;
  const char* sBd = (const char*)wd + boff;
  const char* sBo = (const char*)wo + boff - (size_t)112 * 32 * 2048;
  const unsigned ldsA = lds0 + wid * 1024, ldsB = lds0 + 16384 + wid * 1024;

  auto issue = [&](int i, int slot) {
    const int s = st0 + i;
    const char* pa = sA + (size_t)i * (2 * S_ * 32);
    glds16(pa, voffA, ldsA + slot * G2_STAGE);
    glds16(pa + S_ * 32, voffA, ldsA + slot * G2_STAGE + 8192);
    const char* pb = (s < 112 ? sBd : sBo) + (size_t)s * (32 * 2048);
    glds16(pb, voffB, ldsB + slot * G2_STAGE);
  };
  issue(0, 0); issue(1, 1); issue(2, 2); issue(3, 3);

  const int fr = lane & 15, g4 = lane >> 4;
  const char LAS* rA = lds + (g4 >> 1) * 8192 + (wm * 64 + fr) * 32 + (g4 & 1) * 16;
  const char LAS* rB;
  { const int q = (lane & 15) >> 2, p = lane & 3;
    const int f = ((q >> 1) & 1) | ((g4 & 1) << 1);
    rB = lds + 16384 + wn * 4096 + g4 * 1024 + q * 128 + (f << 5) + p * 8; }
  const char LAS* rBn[4];
#pragma unroll
  for (int nb = 0; nb < 4; ++nb) rBn[nb] = (const char LAS*)(uintptr_t)((unsigned)(uintptr_t)rB ^ (unsigned)(nb << 5));

  float omg[4];
#pragma unroll
  for (int m = 0; m < 4; ++m) omg[m] = 1.0f - ((const float*)(ws + WS_G))[tm * 256 + wm * 64 + m * 16 + fr];

  f32x4 acc[4][4];
#pragma unroll
  for (int m = 0; m < 4; ++m)
#pragma unroll
    for (int n = 0; n < 4; ++n) acc[m][n] = (f32x4){0.f, 0.f, 0.f, 0.f};
  bf16x8 Af[4], Bf[4];
  const int scale_at = (sp == 3) ? 4 : G2_SPLIT_ST;

  WAIT_V(9); BAR;
  if (wn == 1) BAR;
  int slot = 0, islot = 4;
  for (int i = 0; i < G2_SPLIT_ST; ++i) {
    const int so = slot * G2_STAGE;
#pragma unroll
    for (int n = 0; n < 4; ++n) { s16x4 lo = lds_tr(rBn[n] + so), hi = lds_tr(rBn[n] + so + 512); Bf[n] = cat8(lo, hi); }
#pragma unroll
    for (int m = 0; m < 4; ++m) Af[m] = lds_b128(rA + so + m * 512);
    if (i + 4 < G2_SPLIT_ST) { issue(i + 4, islot); WAIT_V(9); }
    else if (i + 3 < G2_SPLIT_ST) { WAIT_V(6); }
    else if (i + 2 < G2_SPLIT_ST) { WAIT_V(3); }
    else { WAIT_V(0); }
    if (i == scale_at) {
#pragma unroll
      for (int m = 0; m < 4; ++m)
#pragma unroll
        for (int n = 0; n < 4; ++n) acc[m][n] *= omg[m];
    }
    BAR; WAIT_L0;
    __builtin_amdgcn_s_setprio(1);
#pragma unroll
    for (int m = 0; m < 4; ++m)
#pragma unroll
      for (int n = 0; n < 4; ++n) acc[m][n] = __builtin_amdgcn_mfma_f32_16x16x32_bf16(Bf[n], Af[m], acc[m][n], 0, 0, 0);
    __builtin_amdgcn_s_setprio(0);
    BAR; SCHED;
    slot = (slot == G2_NST - 1) ? 0 : slot + 1;
    islot = (islot == G2_NST - 1) ? 0 : islot + 1;
  }
  if (wn == 0) BAR;
  if (scale_at == G2_SPLIT_ST) {
#pragma unroll
    for (int m = 0; m < 4; ++m)
#pragma unroll
      for (int n = 0; n < 4; ++n) acc[m][n] *= omg[m];
  }

  auto rs = __builtin_amdgcn_make_buffer_rsrc((void*)(ws + WS_PART), 0, 32u << 20, 0x00020000);
#pragma unroll
  for (int d = 0; d < 4; ++d) {
    if (d != sp) {
#pragma unroll
      for (int n = 0; n < 4; ++n) {
        const unsigned off = ((((unsigned)tile * 4 + d) * 4 + sp) * 4 + n) * 8192u + tid * 16;
        __builtin_amdgcn_raw_buffer_store_b128(__builtin_bit_cast(u32x4, acc[d][n]), rs, off, 0, 16);
      }
    }
  }
  f32x4 own[4];
#pragma unroll
  for (int n = 0; n < 4; ++n) own[n] = acc[0][n];
#pragma unroll
  for (int d = 1; d < 4; ++d)
    if (d == sp) {
#pragma unroll
      for (int n = 0; n < 4; ++n) own[n] = acc[d][n];
    }
  WAIT_V(0);
  __syncthreads();
  unsigned* cnt = (unsigned*)(ws + WS_CNT) + tile * 64;
  if (tid == 0) {
    __hip_atomic_fetch_add(cnt, 1u, __ATOMIC_RELAXED, __HIP_MEMORY_SCOPE_AGENT);
    unsigned spins = 0;
    while (__hip_atomic_load(cnt, __ATOMIC_RELAXED, __HIP_MEMORY_SCOPE_AGENT) < 4u) {
      __builtin_amdgcn_s_sleep(1);
      if (++spins > (1u << 22)) break;
    }
  }
  __syncthreads();
#pragma unroll
  for (int j = 1; j < 4; ++j) {
    const int src = (sp + j) & 3;
#pragma unroll
    for (int n = 0; n < 4; ++n) {
      const unsigned off = ((((unsigned)tile * 4 + sp) * 4 + src) * 4 + n) * 8192u + tid * 16;
      own[n] += __builtin_bit_cast(f32x4, __builtin_amdgcn_raw_buffer_load_b128(rs, off, 0, 16));
    }
  }
  {
    const int row = tm * 256 + wm * 64 + sp * 16 + fr;
    u16* op = out + (size_t)row * D_ + tn * 128 + wn * 64 + 4 * g4;
#pragma unroll
    for (int n = 0; n < 4; ++n) {
      u32x2 w = {cvtpk(own[n][0], own[n][1]), cvtpk(own[n][2], own[n][3])};
      *(u32x2*)(op + n * 16) = w;
    }
  }
}

extern "C" void kernel_launch(void* const* d_in, const int* in_sizes, int n_in, void* d_out, int out_size, void* d_ws, size_t ws_size,
                              hipStream_t stream) {
  const u16* x = (const u16*)d_in[0];
  const u16* wqkv = (const u16*)d_in[1];
  const u16* wo = (const u16*)d_in[2];
  const u16* wg = (const u16*)d_in[4];
  const u16* wu = (const u16*)d_in[5];
  const u16* wd = (const u16*)d_in[6];
  const u16* gate_w = (const u16*)d_in[7];
  char* ws = (char*)d_ws;
  (void)hipFuncSetAttribute((const void*)k_gemm1, hipFuncAttributeMaxDynamicSharedMemorySize, G1_LDS);
  (void)hipFuncSetAttribute((const void*)k_attn, hipFuncAttributeMaxDynamicSharedMemorySize, AT_LDS);
  (void)hipFuncSetAttribute((const void*)k_gemm2, hipFuncAttributeMaxDynamicSharedMemorySize, G2_LDS);
  k_gemm1<<<256, 512, G1_LDS, stream>>>(x, wqkv, wg, wu, gate_w, ws);
  k_attn<<<256, 512, AT_LDS, stream>>>(ws);
  k_gemm2<<<256, 512, G2_LDS, stream>>>(wd, wo, ws, (u16*)d_out);
}
```

```cpp
#include <hip/hip_runtime.h>
#include <hip/hip_bf16.h>
#include <stdint.h>

#define LAS __attribute__((address_space(3)))
typedef unsigned short u16;
typedef short bf16x8 __attribute__((ext_vector_type(8)));
typedef short s16x4 __attribute__((ext_vector_type(4)));
typedef float f32x4 __attribute__((ext_vector_type(4)));
typedef float f32x16 __attribute__((ext_vector_type(16)));
typedef unsigned u32x4 __attribute__((ext_vector_type(4)));
typedef unsigned u32x2 __attribute__((ext_vector_type(2)));

constexpr int S_ = 2048, D_ = 1024, F_ = 3584, NQKV = 3072, K2 = F_ + D_;
constexpr float QSCALE = 0.17677669529663687f * 1.4426950408889634f;

constexpr size_t WS_QKV = 0;
constexpr size_t WS_A2 = WS_QKV + (size_t)NQKV * S_ * 2;
constexpr size_t WS_G = WS_A2 + (size_t)K2 * S_ * 2;
constexpr size_t WS_CNT = WS_G + S_ * 4;
constexpr size_t WS_PART = WS_CNT + 65536;

#define WAIT_V(n) asm volatile("s_waitcnt vmcnt(" #n ")" ::: "memory")
#define WAIT_L0 asm volatile("s_waitcnt lgkmcnt(0)" ::: "memory")
#define BAR __builtin_amdgcn_s_barrier()
#define SCHED __builtin_amdgcn_sched_barrier(0)

#define STAMP_DECL do {} while (0)
#define STAMP(kid, i) do {} while (0)
#define STAMP_END(kid, n) do {} while (0)
__device__ __forceinline__ int fresh_lane() {
  int l;
  asm volatile("v_mbcnt_lo_u32_b32 %0, -1, 0\n\tv_mbcnt_hi_u32_b32 %0, -1, %0" : "=v"(l));
  return l;
}
__device__ __forceinline__ void glds16(const void* sbase, unsigned voff, unsigned lds_addr) {
  asm volatile("s_mov_b32 m0, %2\n\ts_nop 0\n\tglobal_load_lds_dwordx4 %0, %1" ::"v"(voff), "s"(sbase), "s"(lds_addr) : "memory");
}
typedef __bf16 bf16x2_t __attribute__((ext_vector_type(2)));
typedef float f32x2 __attribute__((ext_vector_type(2)));
__device__ __forceinline__ unsigned cvtpk(float lo, float hi) {
  return __builtin_bit_cast(unsigned, __builtin_convertvector((f32x2){lo, hi}, bf16x2_t));
}
__device__ __forceinline__ bf16x8 lds_b128(const char LAS* p) { return *(const LAS bf16x8*)p; }
__device__ __forceinline__ s16x4 lds_tr(const char LAS* p) {
  return __builtin_bit_cast(s16x4, __builtin_amdgcn_ds_read_tr16_b64_v4i16((LAS s16x4*)p));
}
__device__ __forceinline__ bf16x8 cat8(s16x4 a, s16x4 b) {
  return (bf16x8){a[0], a[1], a[2], a[3], b[0], b[1], b[2], b[3]};
}
__device__ __forceinline__ float bf2f(unsigned short v) { return __uint_as_float((unsigned)v << 16); }
__device__ __forceinline__ float gelu_t(float x) {
  const float c0 = -2.0f * 0.7978845608028654f * 1.4426950408889634f;
  const float c1 = c0 * 0.044715f;
  float t = x * x;
  float z = x * __builtin_fmaf(t, c1, c0);
  float e = __builtin_amdgcn_exp2f(z);
  return x * __builtin_amdgcn_rcpf(1.0f + e);
}

constexpr int G1_LDS_A = 0;
constexpr int G1_LDS_B = 65536;
constexpr int G1_LDS_B4 = 131072;
constexpr int G1_LDS = 147456;

#define STMODE 0
__device__ __forceinline__ void ST16(u16* p, u32x4 w) {
  *(u32x4*)p = w;
}
__device__ __forceinline__ void store_pair16(u16* dst_blk  , int row0, int lane, unsigned x0,
                                             unsigned x1, unsigned y0, unsigned y1) {
  auto r0 = __builtin_amdgcn_permlane16_swap(x0, y0, false, false);
  auto r1 = __builtin_amdgcn_permlane16_swap(x1, y1, false, false);
  const int q = lane >> 4;
  const int row = row0 + (q & 1) * 16 + (lane & 15);
  u32x4 w = {r0[0], r1[0], r0[1], r1[1]};
  ST16(dst_blk + (size_t)row * 16 + (q >> 1) * 8, w);
  SCHED;
}

__global__ __launch_bounds__(512, 2) void k_gemm1(const u16* __restrict__ x, const u16* __restrict__ wqkv, const u16* __restrict__ wg,
                                                  const u16* __restrict__ wu, const u16* __restrict__ gate_w, char* __restrict__ ws) {
  extern __shared__ __attribute__((aligned(16))) char lds_[];
  char LAS* lds = (char LAS*)lds_;
  const unsigned lds0 = (unsigned)(uintptr_t)lds;
  const int tid = threadIdx.x, lane = tid & 63;
  const int wid = __builtin_amdgcn_readfirstlane(tid >> 6);
  const int wr = wid >> 2, wc = wid & 3;
  const int bid = blockIdx.x;
  const int xcd = bid & 7, idx = bid >> 3;
#define G1MAP 1
  const int tn = G1MAP ? (xcd + 8 * (idx & 3)) : (xcd * 4 + (idx & 3)), tm = idx >> 2;

  int ptype[5], pcol[5];
  if (tn < 9) {
#pragma unroll
    for (int p = 0; p < 5; ++p) { ptype[p] = 0; pcol[p] = (tn * 5 + p) * 64; }
  } else if (tn == 9) {
    ptype[0] = 1; pcol[0] = 0; ptype[1] = 2; pcol[1] = 0;
    ptype[2] = 0; pcol[2] = 45 * 64; ptype[3] = 0; pcol[3] = 46 * 64; ptype[4] = 0; pcol[4] = 47 * 64;
  } else {
    const int t = tn - 10, F0 = 64 + 160 * t, odd = t & 1;
    const int g0 = F0 + (odd ? 32 : 0), g1 = g0 + 64, fm = odd ? F0 : F0 + 128;
    ptype[0] = 1; pcol[0] = g0; ptype[1] = 2; pcol[1] = g0;
    ptype[2] = 1; pcol[2] = g1; ptype[3] = 2; pcol[3] = g1;
    ptype[4] = 3; pcol[4] = fm;
  }

  const char* sA = (const char*)x + (size_t)(tm * 256 + wid * 8) * 2048;
  unsigned voffA;
  { const int r = lane >> 3, j = lane & 7; voffA = r * 2048 + ((j ^ ((4 * (wid & 1) + (r >> 1)) & 7)) << 4); }
  const unsigned ldsA = lds0 + G1_LDS_A + wid * 1024;
  const int myp = wid >> 1;
  int mt = ptype[0], mc = pcol[0];
#pragma unroll
  for (int p = 1; p < 4; ++p) if (myp == p) { mt = ptype[p]; mc = pcol[p]; }
  const unsigned strB = (mt == 0) ? 6144u : 7168u;
  const char* sB = (const char*)(mt == 0 ? wqkv : (mt == 1 ? wg : wu)) + (size_t)mc * 2 + (size_t)(16 * (wid & 1)) * strB;
  unsigned voffB0, voffB1;
  { const int r = lane >> 3, j = lane & 7, q = r & 3;
    const int f0 = (q >> 1) & 1, f1 = f0 | 2;
    voffB0 = r * strB + ((((j >> 1) ^ f0) << 1 | (j & 1)) << 4);
    voffB1 = (r + 8) * strB + ((((j >> 1) ^ f1) << 1 | (j & 1)) << 4); }
  const unsigned ldsB = lds0 + G1_LDS_B + myp * 8192 + (wid & 1) * 2048;
  const char* sB4; unsigned voffB4, strB4;
  if (ptype[4] == 0) {
    strB4 = 6144u;
    sB4 = (const char*)wqkv + (size_t)pcol[4] * 2 + (size_t)(8 * wid) * strB4;
    const int r = lane >> 3, j = lane & 7, q = r & 3;
    const int f = ((q >> 1) & 1) | ((wid & 1) << 1);
    voffB4 = r * strB4 + ((((j >> 1) ^ f) << 1 | (j & 1)) << 4);
  } else {
    strB4 = 7168u;
    const int isUp = (wid >> 1) & 1;
    sB4 = (const char*)(isUp ? wu : wg) + (size_t)pcol[4] * 2 + (size_t)(32 * (wid >> 2) + 16 * (wid & 1)) * strB4;
    const int t = lane >> 5, glo = (lane >> 4) & 1, q = (lane >> 2) & 3, gr = lane & 3;
    voffB4 = (8 * glo + 4 * t + q) * strB4 + ((gr ^ (glo | (isUp << 1))) << 4);
  }
  const unsigned ldsB4 = lds0 + G1_LDS_B4 + wid * 1024;

#define G1_A0(kt, b) do { glds16(sA + (size_t)(kt) * 128, voffA, ldsA + (b) * 32768); \
                          glds16(sA + (size_t)(kt) * 128 + 64 * 2048, voffA, ldsA + (b) * 32768 + 8192); } while (0)
#define G1_A1(kt, b) do { glds16(sA + (size_t)(kt) * 128 + 128 * 2048, voffA, ldsA + (b) * 32768 + 16384); \
                          glds16(sA + (size_t)(kt) * 128 + 192 * 2048, voffA, ldsA + (b) * 32768 + 24576); } while (0)
#define G1_X(kt, b) do { const char* _p = sB + (size_t)(kt) * 64 * strB; glds16(_p, voffB0, ldsB + (b) * 32768); \
                         glds16(_p, voffB1, ldsB + (b) * 32768 + 1024); } while (0)
#define G1_Y(kt, b) do { const char* _p = sB + (size_t)((kt) * 64 + 32) * strB; glds16(_p, voffB0, ldsB + (b) * 32768 + 4096); \
                         glds16(_p, voffB1, ldsB + (b) * 32768 + 4096 + 1024); } while (0)
#define G1_Z(kt, b) glds16(sB4 + (size_t)(kt) * 64 * strB4, voffB4, ldsB4 + (b) * 8192)

  STAMP_DECL;
  STAMP(0, 0);
  G1_X(0, 0); G1_Z(0, 0); G1_A0(0, 0); G1_A1(0, 0); G1_Y(0, 0); G1_X(1, 1); G1_Z(1, 1);

  const int fr = lane & 15, g4 = lane >> 4;
  const char LAS* rA0 = lds + G1_LDS_A + wr * 8192 + fr * 128 + (((0 + g4) ^ (fr >> 1)) << 4);
  const char LAS* rA1 = lds + G1_LDS_A + wr * 8192 + fr * 128 + (((4 + g4) ^ (fr >> 1)) << 4);
  const char LAS* rB; const char LAS* rB4;
  { const int q = (lane & 15) >> 2, p = lane & 3;
    const int f = ((q >> 1) & 1) | ((g4 & 1) << 1);
    const int o = g4 * 1024 + q * 128 + ((wc ^ f) << 5) + p * 8;
    rB = lds + G1_LDS_B + o;
    const int isUp = p >> 1;
    const int om = (isUp << 11) | ((g4 >> 1) << 10) | ((g4 & 1) << 8) | (q << 6) | ((wc ^ ((g4 & 1) | (isUp << 1))) << 4) | ((p & 1) << 3);
    rB4 = lds + G1_LDS_B4 + (ptype[4] == 0 ? o : om); }

  f32x4 acc[2][4][5];
#pragma unroll
  for (int a = 0; a < 2; ++a)
#pragma unroll
    for (int m = 0; m < 4; ++m)
#pragma unroll
      for (int p = 0; p < 5; ++p) acc[a][m][p] = (f32x4){0.f, 0.f, 0.f, 0.f};
  bf16x8 Af[4], Bf[5];

#define G1_LDB(b, ks) do { _Pragma("unroll") for (int p = 0; p < 4; ++p) { \
      s16x4 lo = lds_tr(rB + (b) * 32768 + p * 8192 + (ks) * 4096), hi = lds_tr(rB + (b) * 32768 + p * 8192 + (ks) * 4096 + 512); Bf[p] = cat8(lo, hi); } \
    { s16x4 lo = lds_tr(rB4 + (b) * 8192 + (ks) * 4096), hi = lds_tr(rB4 + (b) * 8192 + (ks) * 4096 + 512); Bf[4] = cat8(lo, hi); } } while (0)
#define G1_LDA(b, ai, ks) do { _Pragma("unroll") for (int m = 0; m < 4; ++m) \
      Af[m] = lds_b128(((ks) ? rA1 : rA0) + (b) * 32768 + (ai) * 16384 + m * 2048); } while (0)
#define G1_MMA(ai) do { __builtin_amdgcn_s_setprio(1); _Pragma("unroll") for (int m = 0; m < 4; ++m) _Pragma("unroll") for (int p = 0; p < 5; ++p) \
      acc[ai][m][p] = __builtin_amdgcn_mfma_f32_16x16x32_bf16(Bf[p], Af[m], acc[ai][m][p], 0, 0, 0); __builtin_amdgcn_s_setprio(0); } while (0)

  WAIT_V(7); BAR;
  STAMP(0, 1);
  if (wr == 1) BAR;

#define G1_TILE(t, b, I1, I2, W0, W1, W3) do { \
    G1_LDB(b, 0); G1_LDA(b, 0, 0); if (I1) G1_A0((t) + 1, (b) ^ 1); W0; BAR; WAIT_L0; G1_MMA(0); BAR; SCHED; \
    G1_LDA(b, 1, 0); if (I1) { G1_A1((t) + 1, (b) ^ 1); G1_Y((t) + 1, (b) ^ 1); } W1; BAR; WAIT_L0; G1_MMA(1); BAR; SCHED; \
    G1_LDB(b, 1); G1_LDA(b, 0, 1); BAR; WAIT_L0; G1_MMA(0); BAR; SCHED; \
    G1_LDA(b, 1, 1); if (I2) { G1_X((t) + 2, b); G1_Z((t) + 2, b); } W3; BAR; WAIT_L0; G1_MMA(1); BAR; SCHED; \
  } while (0)

  for (int t = 0; t < 14; t += 2) {
    G1_TILE(t, 0, 1, 1, WAIT_V(7), WAIT_V(9), WAIT_V(7));
    G1_TILE(t + 1, 1, 1, 1, WAIT_V(7), WAIT_V(9), WAIT_V(7));
  }
  G1_TILE(14, 0, 1, 0, WAIT_V(7), WAIT_V(9), WAIT_V(4));
  G1_TILE(15, 1, 0, 0, WAIT_V(2), WAIT_V(0), (void)0);
  if (wr == 0) BAR;
  STAMP(0, 2);
  const int lane_e = fresh_lane();
#define lane lane_e

  {
    const int row = bid * 8 + wid;
    const u32x4* xr = (const u32x4*)(x + (size_t)row * D_) + lane * 2;
    const u32x4* gw = (const u32x4*)gate_w + lane * 2;
    u32x4 a0 = xr[0], a1 = xr[1], b0 = gw[0], b1 = gw[1];
    float s = 0.f;
#pragma unroll
    for (int i = 0; i < 4; ++i) {
      s += __uint_as_float(a0[i] << 16) * __uint_as_float(b0[i] << 16) + __uint_as_float(a0[i] & 0xffff0000u) * __uint_as_float(b0[i] & 0xffff0000u);
      s += __uint_as_float(a1[i] << 16) * __uint_as_float(b1[i] << 16) + __uint_as_float(a1[i] & 0xffff0000u) * __uint_as_float(b1[i] & 0xffff0000u);
    }
#pragma unroll
    for (int o = 32; o >= 1; o >>= 1) s += __shfl_xor(s, o);
    if (lane == 0) ((float*)(ws + WS_G))[row] = 1.0f / (1.0f + __expf(-s));
    if (wid == 0 && lane == 0) ((unsigned*)(ws + WS_CNT))[(16 + bid) * 32] = 0u;
    if (wid == 1 && lane == 0 && bid < 16) ((unsigned*)(ws + WS_CNT))[bid * 32] = 0u;
  }

  u16* qkv = (u16*)(ws + WS_QKV);
  u16* a2 = (u16*)(ws + WS_A2);
  const int rowb = tm * 256 + wr * 64;
#pragma unroll
  for (int pp = 0; pp < 2; ++pp) {
    if (ptype[2 * pp] == 0) {
#pragma unroll
      for (int h = 0; h < 2; ++h) {
        const int p = 2 * pp + h;
        const float sc = pcol[p] < D_ ? QSCALE : 1.0f;
        u16* dst = qkv + (size_t)(pcol[p] / 16 + wc) * S_ * 16;
#pragma unroll
        for (int a = 0; a < 2; ++a)
#pragma unroll
          for (int m = 0; m < 4; m += 2) {
            const f32x4 u = acc[a][m][p] * sc, v = acc[a][m + 1][p] * sc;
            store_pair16(dst, rowb + a * 128 + m * 16, lane, cvtpk(u[0], u[1]), cvtpk(u[2], u[3]), cvtpk(v[0], v[1]), cvtpk(v[2], v[3]));
          }
      }
    } else {
      u16* dst = a2 + (size_t)(pcol[2 * pp] / 16 + wc) * S_ * 16;
#pragma unroll
      for (int a = 0; a < 2; ++a)
#pragma unroll
        for (int m = 0; m < 4; m += 2) {
          f32x4 u, v;
#pragma unroll
          for (int j = 0; j < 4; ++j) {
            u[j] = gelu_t(acc[a][m][2 * pp][j]) * acc[a][m][2 * pp + 1][j];
            v[j] = gelu_t(acc[a][m + 1][2 * pp][j]) * acc[a][m + 1][2 * pp + 1][j];
          }
          store_pair16(dst, rowb + a * 128 + m * 16, lane, cvtpk(u[0], u[1]), cvtpk(u[2], u[3]), cvtpk(v[0], v[1]), cvtpk(v[2], v[3]));
        }
    }
  }
  if (ptype[4] == 0) {
    const float sc = pcol[4] < D_ ? QSCALE : 1.0f;
    u16* dst = qkv + (size_t)(pcol[4] / 16 + wc) * S_ * 16;
#pragma unroll
    for (int a = 0; a < 2; ++a)
#pragma unroll
      for (int m = 0; m < 4; m += 2) {
        const f32x4 u = acc[a][m][4] * sc, v = acc[a][m + 1][4] * sc;
        store_pair16(dst, rowb + a * 128 + m * 16, lane, cvtpk(u[0], u[1]), cvtpk(u[2], u[3]), cvtpk(v[0], v[1]), cvtpk(v[2], v[3]));
      }
  } else {
    u16* dst = a2 + (size_t)((pcol[4] + 8 * wc) / 16) * S_ * 16 + (wc & 1) * 8;
#pragma unroll
    for (int a = 0; a < 2; ++a) {
      unsigned pk[2][2];
#pragma unroll
      for (int mp = 0; mp < 2; ++mp) {
        float hv[4];
#pragma unroll
        for (int j = 0; j < 4; ++j) {
          auto r = __builtin_amdgcn_permlane32_swap(__float_as_uint(acc[a][2 * mp][4][j]), __float_as_uint(acc[a][2 * mp + 1][4][j]), false, false);
          hv[j] = gelu_t(__uint_as_float(r[0])) * __uint_as_float(r[1]);
        }
        pk[mp][0] = cvtpk(hv[0], hv[1]); pk[mp][1] = cvtpk(hv[2], hv[3]);
      }
      auto r0 = __builtin_amdgcn_permlane16_swap(pk[0][0], pk[1][0], false, false);
      auto r1 = __builtin_amdgcn_permlane16_swap(pk[0][1], pk[1][1], false, false);
      const int q = lane >> 4;
      const int row = rowb + a * 128 + ((q & 1) * 2 + (q >> 1)) * 16 + (lane & 15);
      u32x4 w = {r0[0], r1[0], r0[1], r1[1]};
      ST16(dst + (size_t)row * 16, w);
      SCHED;
    }
  }
#undef lane
  STAMP(0, 3);
  STAMP(0, 4);
  STAMP_END(0, 5);
}

constexpr int K2_SLOT = 24576;
constexpr int K2_G = 6 * K2_SLOT;
constexpr int K2_LDS = K2_G + 1024;
constexpr int AT_K = 3 * K2_SLOT;
constexpr int AT_V = AT_K + 32768;
constexpr int AT_V1 = AT_V + 16384 + 128;
static_assert(AT_V1 + 16384 <= K2_LDS, "lds");
constexpr int NH = 28, NC = 16, NS = NH + NC;

__device__ __forceinline__ void st16_sc1(void* p, u32x4 w) {
  asm volatile("global_store_dwordx4 %0, %1, off sc1\n\ts_nop 1" ::"v"(p), "v"(w) : "memory");
}
template <int SC1>
__device__ __forceinline__ void glds16x4(const void* sbase, unsigned voff, unsigned lds_addr) {
  if (SC1)
    asm volatile("s_mov_b32 m0, %2\n\ts_nop 0\n\tglobal_load_lds_dwordx4 %0, %1 sc1\n\tglobal_load_lds_dwordx4 %0, %1 offset:1024 sc1\n\t"
                 "global_load_lds_dwordx4 %0, %1 offset:2048 sc1\n\tglobal_load_lds_dwordx4 %0, %1 offset:3072 sc1" ::"v"(voff), "s"(sbase), "s"(lds_addr) : "memory");
  else
    asm volatile("s_mov_b32 m0, %2\n\ts_nop 0\n\tglobal_load_lds_dwordx4 %0, %1\n\tglobal_load_lds_dwordx4 %0, %1 offset:1024\n\t"
                 "global_load_lds_dwordx4 %0, %1 offset:2048\n\tglobal_load_lds_dwordx4 %0, %1 offset:3072" ::"v"(voff), "s"(sbase), "s"(lds_addr) : "memory");
}
__device__ __forceinline__ void glds16x2(const void* sbase, unsigned voff0, unsigned voff1m, unsigned lds_addr) {
  asm volatile("s_mov_b32 m0, %3\n\ts_nop 0\n\tglobal_load_lds_dwordx4 %0, %2\n\tglobal_load_lds_dwordx4 %1, %2 offset:1024" ::"v"(voff0), "v"(voff1m),
               "s"(sbase), "s"(lds_addr) : "memory");
}
#define LP(a) ((const char LAS*)(uintptr_t)(a))

template <bool FIRST>
__device__ __forceinline__ void attn_wave(const char LAS* lds, char* ws, int wid, int lane, int head, int qrow, const bf16x8 (&qf)[2], float gval) {
  const int r32 = lane & 31, hi = lane >> 5;
  const char LAS* kbase = lds + AT_K + wid * 1024 + r32 * 32 + ((hi ^ ((r32 >> 3) & 1)) << 4);
  const int kb0 = FIRST ? (8 - wid) : 0;
  f32x16 s[9];
  f32x16 m0, m8;
#pragma unroll
  for (int r = 0; r < 16; ++r) {
    const int c = (r & 3) + 8 * (r >> 2) + 4 * hi;
    m0[r] = (c > r32) ? 0.f : -INFINITY;
    m8[r] = (c > r32) ? -INFINITY : 0.f;
  }
  const f32x16 zero = {0.f, 0.f, 0.f, 0.f, 0.f, 0.f, 0.f, 0.f, 0.f, 0.f, 0.f, 0.f, 0.f, 0.f, 0.f, 0.f};
#pragma unroll
  for (int kb = 0; kb < 9; ++kb) {
    f32x16 z = (kb == 0) ? m0 : ((kb == 8) ? m8 : zero);
    if (!FIRST || kb >= kb0) {
      bf16x8 k0 = lds_b128(kbase + kb * 1024), k1 = lds_b128(kbase + 16384 + kb * 1024);
      z = __builtin_amdgcn_mfma_f32_32x32x16_bf16(k0, qf[0], z, 0, 0, 0);
      z = __builtin_amdgcn_mfma_f32_32x32x16_bf16(k1, qf[1], z, 0, 0, 0);
    } else {
#pragma unroll
      for (int r = 0; r < 16; ++r) z[r] = -INFINITY;
    }
    s[kb] = z;
  }
  float ma = s[8][0], mb = s[8][1];
#pragma unroll
  for (int kb = 0; kb < 9; ++kb)
#pragma unroll
    for (int r = 0; r < 16; r += 4) {
      ma = __builtin_fmaxf(__builtin_fmaxf(ma, s[kb][r]), s[kb][r + 1]);
      mb = __builtin_fmaxf(__builtin_fmaxf(mb, s[kb][r + 2]), s[kb][r + 3]);
    }
  float m = __builtin_fmaxf(ma, mb);
  { auto rr = __builtin_amdgcn_permlane32_swap(__float_as_uint(m), __float_as_uint(m), false, false);
    m = __builtin_fmaxf(__uint_as_float(rr[0]), __uint_as_float(rr[1])); }
  f32x16 o = zero;
  f32x4 lsum = {0.f, 0.f, 0.f, 0.f};
  bf16x8 sel;
  { const short one = (((lane >> 4) & 1) == (lane & 1)) ? (short)0x3F80 : (short)0;
    sel = (bf16x8){one, one, one, one, one, one, one, one}; }
  const char LAS* vbase;
  { const int g = lane >> 4, q = (lane & 15) >> 2, p = lane & 3;
    vbase = lds + ((g & 1) ? AT_V1 : AT_V) + wid * 1024 + (4 * (g >> 1) + q) * 32 + p * 8; }
#pragma unroll
  for (int kb = 0; kb < 9; ++kb) {
    if (!FIRST || kb >= kb0) {
      unsigned pk[8];
#pragma unroll
      for (int r = 0; r < 16; r += 2) {
        const float e0 = __builtin_amdgcn_exp2f(s[kb][r] - m), e1 = __builtin_amdgcn_exp2f(s[kb][r + 1] - m);
        pk[r >> 1] = cvtpk(e0, e1);
      }
#pragma unroll
      for (int st = 0; st < 2; ++st) {
        s16x4 lo = lds_tr(vbase + kb * 1024 + st * 512), hi4 = lds_tr(vbase + kb * 1024 + st * 512 + 256);
        u32x4 pw = {pk[4 * st], pk[4 * st + 1], pk[4 * st + 2], pk[4 * st + 3]};
        o = __builtin_amdgcn_mfma_f32_32x32x16_bf16(cat8(lo, hi4), __builtin_bit_cast(bf16x8, pw), o, 0, 0, 0);
        lsum = __builtin_amdgcn_mfma_f32_16x16x32_bf16(sel, __builtin_bit_cast(bf16x8, pw), lsum, 0, 0, 0);
      }
    }
  }
  const float l = (lane & 16) ? lsum[1] : lsum[0];
  const float sc = gval / l;
  u16* a2 = (u16*)(ws + WS_A2);
#pragma unroll
  for (int dh = 0; dh < 2; ++dh) {
    const unsigned a0 = cvtpk(o[8 * dh + 0] * sc, o[8 * dh + 1] * sc), a1 = cvtpk(o[8 * dh + 2] * sc, o[8 * dh + 3] * sc);
    const unsigned b0 = cvtpk(o[8 * dh + 4] * sc, o[8 * dh + 5] * sc), b1 = cvtpk(o[8 * dh + 6] * sc, o[8 * dh + 7] * sc);
    auto r0 = __builtin_amdgcn_permlane32_swap(a0, b0, false, false);
    auto r1 = __builtin_amdgcn_permlane32_swap(a1, b1, false, false);
    u32x4 w = {r0[0], r1[0], r0[1], r1[1]};
    st16_sc1(a2 + ((size_t)(224 + head * 2 + dh) * S_ + qrow) * 16 + hi * 8, w);
  }
}

__device__ __forceinline__ void wait_count(unsigned* c, unsigned want) {
  unsigned spins = 0;
  while ((unsigned)__builtin_amdgcn_readfirstlane(__hip_atomic_load(c, __ATOMIC_RELAXED, __HIP_MEMORY_SCOPE_AGENT)) < want) {
    __builtin_amdgcn_s_sleep(1);
    if (++spins > (1u << 22)) break;
  }
  asm volatile("" ::: "memory");
}

__global__ __launch_bounds__(512, 2) void k2_fused(const u16* __restrict__ wd, const u16* __restrict__ wo, char* __restrict__ ws,
                                                   u16* __restrict__ out) {
  extern __shared__ __attribute__((aligned(16))) char lds_[];
  char LAS* lds = (char LAS*)lds_;
  const unsigned lds0 = (unsigned)(uintptr_t)lds;
  const int tid = threadIdx.x, lane = tid & 63;
  const int wid = __builtin_amdgcn_readfirstlane(tid >> 6);
  const int bid = blockIdx.x;
  const int xcd = bid & 7, idx = bid >> 3;
  const int sp = xcd & 3, tn = idx & 7, tm = (xcd >> 2) * 4 + (idx >> 3);
  const int mp = sp >> 1, np = sp & 1;
  const int tile = tm * 8 + tn;
  const int head = xcd * 4 + (idx & 3), rb = idx >> 2;
  const int q0 = rb * 256;
  const char* qkv = ws + WS_QKV;
  unsigned* cntA = (unsigned*)(ws + WS_CNT);
  unsigned* cntP = (unsigned*)(ws + WS_CNT + 16 * 128);
  STAMP_DECL;
  STAMP(1, 0);

  {
    const unsigned voffK = (lane >> 1) * 32 + (((lane & 1) ^ ((lane >> 4) & 1)) << 4);
    const unsigned voffV = lane * 16;
#pragma unroll
    for (int i = 1; i >= 0; --i) {
      const int rp = wid + 8 * i;
      if (rb > 0 || i == 1) {
        const int row = q0 - 256 + rp * 32;
#pragma unroll
        for (int dh = 0; dh < 2; ++dh) glds16(qkv + ((size_t)(64 + head * 2 + dh) * S_ + row) * 32, voffK, lds0 + AT_K + dh * 16384 + rp * 1024);
      }
    }
#pragma unroll
    for (int i = 1; i >= 0; --i) {
      const int rp = wid + 8 * i;
      if (rb > 0 || i == 1) {
        const int row = q0 - 256 + rp * 32;
#pragma unroll
        for (int dh = 0; dh < 2; ++dh) glds16(qkv + ((size_t)(128 + head * 2 + dh) * S_ + row) * 32, voffV, lds0 + (dh ? AT_V1 : AT_V) + rp * 1024);
      }
    }
  }
  const int qrow = q0 + wid * 32 + (lane & 31);
  bf16x8 qf[2];
#pragma unroll
  for (int dh = 0; dh < 2; ++dh) qf[dh] = *(const bf16x8*)(qkv + ((size_t)(head * 2 + dh) * S_ + qrow) * 32 + (lane >> 5) * 16);
  const float gval = ((const float*)(ws + WS_G))[qrow];
  const float gt = ((const float*)(ws + WS_G))[tm * 256 + (tid & 255)];
  asm volatile("s_waitcnt vmcnt(0)" : "+v"(qf[0]), "+v"(qf[1]) :: "memory");
  *(LAS float*)(lds + K2_G + (tid & 255) * 4) = 1.0f - gt;

  const int ld = wid & 3;
  unsigned voffA, voffB0, voffB1m;
  auto set_voff = [&]() {
    const int l_ = fresh_lane();
    const int r = l_ >> 3, j = l_ & 7, q = r & 3;
    const int f0 = (q >> 1) & 1, f1 = f0 | 2;
    voffA = l_ * 16;
    voffB0 = r * 2048 + ((((j >> 1) ^ f0) << 1 | (j & 1)) << 4);
    voffB1m = (r + 8) * 2048 + ((((j >> 1) ^ f1) << 1 | (j & 1)) << 4) - 1024;
  };
  const char* hA = ws + WS_A2 + ((size_t)(sp * NH * 2 + (ld >> 1)) * S_ + tm * 256 + ((ld & 1) ^ mp) * 128) * 32;
  const char* hB = (const char*)wd + (size_t)(sp * NH * 32 + 16 * (ld & 1)) * 2048 + (size_t)(tn * 128 + ((ld >> 1) ^ np) * 64) * 2;
  const char* cA = ws + WS_A2 + ((size_t)(224 + ld) * S_ + tm * 256 + mp * 128) * 32;
  const char* cB = (const char*)wo + (size_t)(16 * ld) * 2048 + (size_t)(tn * 128 + np * 64) * 2;
  auto issue = [&](int i) {
    const unsigned dst = lds0 + (i % 6) * K2_SLOT;
    if (i < NH) {
      glds16x4<0>(hA + (size_t)i * (2 * S_ * 32), voffA, dst + ld * 4096);
      glds16x2(hB + (size_t)i * (32 * 2048), voffB0, voffB1m, dst + 16384 + ld * 2048);
    } else {
      glds16x4<1>(cA + (size_t)(i - NH) * (4 * S_ * 32), voffA, dst + ld * 4096);
      glds16x2(cB + (size_t)(i - NH) * (64 * 2048), voffB0, voffB1m, dst + 16384 + ld * 2048);
    }
  };
  if (wid >= 4) { set_voff(); issue(0); issue(1); issue(2); }
  BAR;
  asm volatile("" ::: "memory");
  STAMP(1, 1);

  if (rb == 0) attn_wave<true>(lds, ws, wid, lane, head, qrow, qf, gval);
  else attn_wave<false>(lds, ws, wid, lane, head, qrow, qf, gval);
  STAMP(1, 2);

  if (wid >= 4) {
    set_voff();
    WAIT_V(2);
    WAIT_L0; BAR;
    issue(3); issue(4);
    BAR;
    unsigned* ca = cntA + (tm * 2 + mp) * 32;
    unsigned fl = 0;
    for (int P = 0; P < NS / 2; ++P) {
      const int a = 2 * P + 5;
      if (P == 9) asm volatile("global_load_dword %0, %1, off sc1" : "=v"(fl) : "v"(ca) : "memory");
      if (P == 11) {
        asm volatile("" : "+v"(fl));
        if ((unsigned)__builtin_amdgcn_readfirstlane(fl) < 128u) wait_count(ca, 128u);
      }
      if (a + 1 < NS) { issue(a); issue(a + 1); WAIT_V(12); }
      else if (a < NS) { issue(a); WAIT_V(6); }
      else { WAIT_V(0); }
      if (P == 0 && voffA == 0) __hip_atomic_fetch_add(cntA + (rb * 2 + 1) * 32, 1u, __ATOMIC_RELAXED, __HIP_MEMORY_SCOPE_AGENT);
      if (P + 1 < NS / 2) BAR;
    }
    STAMP(1, 3); STAMP(1, 4); STAMP(1, 5); STAMP(1, 6);
    STAMP_END(1, 7);
    return;
  }

  const int wm = wid & 1, wn = wid >> 1;
  const int fr = lane & 15, g4 = lane >> 4;
  const unsigned rAh = lds0 + (g4 >> 1) * 8192 + (wm * 64 + fr) * 32 + (g4 & 1) * 16;
  unsigned rAc = 0;
  unsigned rB0, rB1;
  { const int q = (lane & 15) >> 2, p = lane & 3;
    const int f = ((q >> 1) & 1) | ((g4 & 1) << 1);
    rB0 = lds0 + 16384 + g4 * 1024 + q * 128 + (((wn * 2) ^ f) << 5) + p * 8;
    rB1 = rB0 ^ 32u; }

  f32x4 acc[8][4];
#pragma unroll
  for (int m = 0; m < 8; ++m)
#pragma unroll
    for (int n = 0; n < 4; ++n) acc[m][n] = (f32x4){0.f, 0.f, 0.f, 0.f};
  bf16x8 A0[8], A1[8];
  s16x4 L0[4], H0[4], L1[4], H1[4];
#define RD(AF, BL, BH, k, so, COV) do { \
    if ((k) < 8) { const int n_ = (k) >> 1; const unsigned b_ = ((n_ & 1) ? rB1 : rB0) + (so); \
      if ((k) & 1) BH[n_] = lds_tr(LP(b_) + (n_ >> 1) * 4096 + 512); else BL[n_] = lds_tr(LP(b_) + (n_ >> 1) * 4096); } \
    else { const int m_ = (k) - 8; \
      if (COV) AF[m_] = lds_b128(LP(rAc + (so)) + (m_ >> 2) * 8192 + (m_ & 3) * 512); \
      else AF[m_] = lds_b128(LP(rAh + (so)) + (m_ >> 2) * 4096 + (m_ & 3) * 512); } } while (0)
#define MFH(AF, BL, BH, k) acc[(k) >> 2][(k) & 3] = __builtin_amdgcn_mfma_f32_16x16x32_bf16(cat8(BL[(k) & 3], BH[(k) & 3]), AF[(k) >> 2], acc[(k) >> 2][(k) & 3], 0, 0, 0)
#define STAGE_H(AC, LC, HC, AN, LN, HN, so, COV) do { \
    _Pragma("unroll") for (int k = 0; k < 32; ++k) { MFH(AC, LC, HC, k); if (k >= 1 && k < 17) RD(AN, LN, HN, k - 1, so, COV); SCHED; } } while (0)

  WAIT_L0; BAR;
  asm volatile("" ::: "memory");
#pragma unroll
  for (int k = 0; k < 16; ++k) RD(A0, L0, H0, k, 0, 0);
  WAIT_L0; BAR;
  SCHED;
  STAMP(1, 3);
  int s1 = 1;
  for (int P = 0; P < NH / 2 - 1; ++P) {
    const int s2 = (s1 == 5) ? 0 : s1 + 1;
    STAGE_H(A0, L0, H0, A1, L1, H1, s1 * K2_SLOT, 0);
    STAGE_H(A1, L1, H1, A0, L0, H0, s2 * K2_SLOT, 0);
    s1 = s2 + 1;
    if (P == 2) { WAIT_V(0); if (lane == 0) __hip_atomic_fetch_add(cntA + (rb * 2) * 32, 1u, __ATOMIC_RELAXED, __HIP_MEMORY_SCOPE_AGENT); }
    WAIT_L0; BAR; SCHED;
  }
  STAGE_H(A0, L0, H0, A1, L1, H1, 3 * K2_SLOT, 0);
  { const int l2 = fresh_lane(); rAc = lds0 + (l2 >> 5) * 4096 + (wm * 64 + (l2 & 15)) * 32 + ((l2 >> 4) & 1) * 16; }
  STAGE_H(A1, L1, H1, A0, L0, H0, 4 * K2_SLOT, 1);
  WAIT_L0; BAR; SCHED;
  STAMP(1, 4);

  auto rs = __builtin_amdgcn_make_buffer_rsrc((void*)(ws + WS_PART), 0, 16u << 20, 0x00020000);
  float omg[8];
  const int l3 = fresh_lane();
#pragma unroll
  for (int m = 0; m < 8; ++m) omg[m] = *(const LAS float*)(lds + K2_G + (((m >> 2) ^ mp) * 128 + wm * 64 + (m & 3) * 16 + (l3 & 15)) * 4);
#pragma unroll
  for (int d = 3; d >= 1; --d) {
    const int h = d >> 1, c = d & 1;
#pragma unroll
    for (int mb = 0; mb < 4; ++mb) {
      const float sc = omg[4 * h + mb];
      const f32x4 u = acc[4 * h + mb][2 * c] * sc, v = acc[4 * h + mb][2 * c + 1] * sc;
      u32x4 w = {cvtpk(u[0], u[1]), cvtpk(u[2], u[3]), cvtpk(v[0], v[1]), cvtpk(v[2], v[3])};
      const unsigned off = (((((unsigned)tile * 4 + (d ^ sp)) * 4 + sp) * 4 + wid) * 4 + mb) * 1024u + l3 * 16;
      __builtin_amdgcn_raw_buffer_store_b128(w, rs, off, 0, 16);
    }
  }
  f32x4 acK[4][2];
#pragma unroll
  for (int mb = 0; mb < 4; ++mb)
#pragma unroll
    for (int j = 0; j < 2; ++j) acK[mb][j] = acc[mb][j] * omg[mb];
  SCHED;
#define MFC(AF, BL, BH, k) acK[((k) >> 1) & 3][(k) & 1] = __builtin_amdgcn_mfma_f32_16x16x32_bf16(cat8(BL[2 * ((k) >> 3) + ((k) & 1)], BH[2 * ((k) >> 3) + ((k) & 1)]), \
                                                             AF[4 * ((k) >> 3) + (((k) >> 1) & 3)], acK[((k) >> 1) & 3][(k) & 1], 0, 0, 0)
#define STAGE_C(AC, LC, HC, AN, LN, HN, so) do { \
    _Pragma("unroll") for (int k = 0; k < 16; ++k) { MFC(AC, LC, HC, k); RD(AN, LN, HN, k, so, 1); SCHED; } } while (0)
  u32x4 part[3][4];
  unsigned* myc = cntP + (tile * 4 + wid) * 32;
  unsigned flagv = 0;
#pragma unroll
  for (int P = NH / 2; P < NS / 2; ++P) {
    STAGE_C(A0, L0, H0, A1, L1, H1, ((2 * P + 1) % 6) * K2_SLOT);
    if (P + 1 < NS / 2) { STAGE_C(A1, L1, H1, A0, L0, H0, ((2 * P + 2) % 6) * K2_SLOT); }
    else {
#pragma unroll
      for (int k = 0; k < 16; ++k) MFC(A1, L1, H1, k);
    }
    if (P == NH / 2 + 2) { WAIT_V(0); if (l3 == 0) __hip_atomic_fetch_add(myc, 1u, __ATOMIC_RELAXED, __HIP_MEMORY_SCOPE_AGENT); }
    if (P == NS / 2 - 4) flagv = __hip_atomic_load(myc, __ATOMIC_RELAXED, __HIP_MEMORY_SCOPE_AGENT);
    if (P == NS / 2 - 3) {
      if ((unsigned)__builtin_amdgcn_readfirstlane(flagv) < 4u) wait_count(myc, 4u);
      asm volatile("" ::: "memory");
#pragma unroll
      for (int jj = 0; jj < 3; ++jj) {
        const int src = (sp + 1 + jj) & 3;
#pragma unroll
        for (int mb = 0; mb < 4; ++mb) {
          const unsigned off = (((((unsigned)tile * 4 + sp) * 4 + src) * 4 + wid) * 4 + mb) * 1024u + l3 * 16;
          part[jj][mb] = __builtin_amdgcn_raw_buffer_load_b128(rs, off, 0, 16);
        }
      }
    }
    if (P + 1 < NS / 2) { WAIT_L0; BAR; SCHED; }
  }
  STAMP(1, 5);
  const int lane_e = fresh_lane();
#pragma unroll
  for (int mb = 0; mb < 4; ++mb) {
    f32x4 u = acK[mb][0], v = acK[mb][1];
#pragma unroll
    for (int jj = 0; jj < 3; ++jj) {
      const u32x4 w = part[jj][mb];
      u[0] += __uint_as_float(w[0] << 16); u[1] += __uint_as_float(w[0] & 0xffff0000u);
      u[2] += __uint_as_float(w[1] << 16); u[3] += __uint_as_float(w[1] & 0xffff0000u);
      v[0] += __uint_as_float(w[2] << 16); v[1] += __uint_as_float(w[2] & 0xffff0000u);
      v[2] += __uint_as_float(w[3] << 16); v[3] += __uint_as_float(w[3] & 0xffff0000u);
    }
    auto r0 = __builtin_amdgcn_permlane16_swap(cvtpk(u[0], u[1]), cvtpk(v[0], v[1]), false, false);
    auto r1 = __builtin_amdgcn_permlane16_swap(cvtpk(u[2], u[3]), cvtpk(v[2], v[3]), false, false);
    u32x4 w = {r0[0], r1[0], r0[1], r1[1]};
    const int row = tm * 256 + mp * 128 + wm * 64 + mb * 16 + (lane_e & 15);
    const int col = tn * 128 + np * 64 + wn * 32 + ((lane_e >> 4) & 1) * 16 + (lane_e >> 5) * 8;
    *(u32x4*)(out + (size_t)row * D_ + col) = w;
  }
  STAMP(1, 6);
  STAMP_END(1, 7);
}

extern "C" void kernel_launch(void* const* d_in, const int* in_sizes, int n_in, void* d_out, int out_size, void* d_ws, size_t ws_size,
                              hipStream_t stream) {
  const u16* x = (const u16*)d_in[0];
  const u16* wqkv = (const u16*)d_in[1];
  const u16* wo = (const u16*)d_in[2];
  const u16* wg = (const u16*)d_in[4];
  const u16* wu = (const u16*)d_in[5];
  const u16* wd = (const u16*)d_in[6];
  const u16* gate_w = (const u16*)d_in[7];
  char* ws = (char*)d_ws;
  (void)hipFuncSetAttribute((const void*)k_gemm1, hipFuncAttributeMaxDynamicSharedMemorySize, G1_LDS);
  (void)hipFuncSetAttribute((const void*)k2_fused, hipFuncAttributeMaxDynamicSharedMemorySize, K2_LDS);
  k_gemm1<<<256, 512, G1_LDS, stream>>>(x, wqkv, wg, wu, gate_w, ws);
  k2_fused<<<256, 512, K2_LDS, stream>>>(wd, wo, ws, (u16*)d_out);
}
```

```cpp
#include <hip/hip_runtime.h>
#include <hip/hip_bf16.h>
#include <stdint.h>

#define LAS __attribute__((address_space(3)))
typedef unsigned short u16;
typedef short bf16x8 __attribute__((ext_vector_type(8)));
typedef short s16x4 __attribute__((ext_vector_type(4)));
typedef float f32x4 __attribute__((ext_vector_type(4)));
typedef float f32x16 __attribute__((ext_vector_type(16)));
typedef unsigned u32x4 __attribute__((ext_vector_type(4)));
typedef unsigned u32x2 __attribute__((ext_vector_type(2)));

constexpr int S_ = 2048, D_ = 1024, F_ = 3584, NQKV = 3072, K2 = F_ + D_;
constexpr float QSCALE = 0.17677669529663687f * 1.4426950408889634f;

constexpr size_t WS_QKV = 0;
constexpr size_t WS_A2 = WS_QKV + (size_t)NQKV * S_ * 2;
constexpr size_t WS_G = WS_A2 + (size_t)K2 * S_ * 2;
constexpr size_t WS_CNT = WS_G + S_ * 4;
constexpr size_t WS_PART = WS_CNT + 65536;
constexpr size_t WS_TOUCH_END = (WS_PART + (16u << 20) + 32767) / 32768 * 32768;

#define WAIT_V(n) asm volatile("s_waitcnt vmcnt(" #n ")" ::: "memory")
#define WAIT_L0 asm volatile("s_waitcnt lgkmcnt(0)" ::: "memory")
#define BAR __builtin_amdgcn_s_barrier()
#define SCHED __builtin_amdgcn_sched_barrier(0)

#define STAMP_DECL do {} while (0)
#define STAMP(kid, i) do {} while (0)
#define STAMP_END(kid, n) do {} while (0)
__device__ __forceinline__ int fresh_lane() {
  int l;
  asm volatile("v_mbcnt_lo_u32_b32 %0, -1, 0\n\tv_mbcnt_hi_u32_b32 %0, -1, %0" : "=v"(l));
  return l;
}
__device__ __forceinline__ void glds16(const void* sbase, unsigned voff, unsigned lds_addr) {
  asm volatile("s_mov_b32 m0, %2\n\ts_nop 0\n\tglobal_load_lds_dwordx4 %0, %1" ::"v"(voff), "s"(sbase), "s"(lds_addr) : "memory");
}
typedef __bf16 bf16x2_t __attribute__((ext_vector_type(2)));
typedef float f32x2 __attribute__((ext_vector_type(2)));
__device__ __forceinline__ unsigned cvtpk(float lo, float hi) {
  return __builtin_bit_cast(unsigned, __builtin_convertvector((f32x2){lo, hi}, bf16x2_t));
}
__device__ __forceinline__ bf16x8 lds_b128(const char LAS* p) { return *(const LAS bf16x8*)p; }
__device__ __forceinline__ s16x4 lds_tr(const char LAS* p) {
  return __builtin_bit_cast(s16x4, __builtin_amdgcn_ds_read_tr16_b64_v4i16((LAS s16x4*)p));
}
__device__ __forceinline__ bf16x8 cat8(s16x4 a, s16x4 b) {
  return (bf16x8){a[0], a[1], a[2], a[3], b[0], b[1], b[2], b[3]};
}
__device__ __forceinline__ float bf2f(unsigned short v) { return __uint_as_float((unsigned)v << 16); }
__device__ __forceinline__ float gelu_t(float x) {
  const float c0 = -2.0f * 0.7978845608028654f * 1.4426950408889634f;
  const float c1 = c0 * 0.044715f;
  float t = x * x;
  float z = x * __builtin_fmaf(t, c1, c0);
  float e = __builtin_amdgcn_exp2f(z);
  return x * __builtin_amdgcn_rcpf(1.0f + e);
}

__device__ __forceinline__ f32x2 geglu2(f32x2 x, f32x2 u) {
  const float c0 = -2.0f * 0.7978845608028654f * 1.4426950408889634f;
  const float c1 = c0 * 0.044715f;
  const f32x2 t = x * x;
  const f32x2 z = x * (t * c1 + c0);
  f32x2 e; e.x = __builtin_amdgcn_exp2f(z.x); e.y = __builtin_amdgcn_exp2f(z.y);
  const f32x2 d = e + 1.0f;
  f32x2 r; r.x = __builtin_amdgcn_rcpf(d.x); r.y = __builtin_amdgcn_rcpf(d.y);
  return (x * u) * r;
}
__device__ __forceinline__ void glds4(const void* sbase, unsigned voff, unsigned lds_addr) {
  asm volatile("s_mov_b32 m0, %2\n\ts_nop 0\n\tglobal_load_lds_dword %0, %1" ::"v"(voff), "s"(sbase), "s"(lds_addr) : "memory");
}

constexpr int G1_LDS_A = 0;
constexpr int G1_LDS_B = 65536;
constexpr int G1_LDS_B4 = 131072;
constexpr int G1_LDS_GW = 147456;
constexpr int G1_LDS_DUMMY = G1_LDS_GW + 2048;
constexpr int G1_LDS = G1_LDS_DUMMY + 2048;

#define QMODE 0
#define HMODE 0
#define TBACK 0
template <int MODE, int OFF>
__device__ __forceinline__ void store_pair16(const void* sbase, unsigned voff, unsigned x0, unsigned x1, unsigned y0, unsigned y1) {
  auto r0 = __builtin_amdgcn_permlane16_swap(x0, y0, false, false);
  auto r1 = __builtin_amdgcn_permlane16_swap(x1, y1, false, false);
  u32x4 w = {r0[0], r1[0], r0[1], r1[1]};
  if (MODE == 0) asm volatile("global_store_dwordx4 %0, %1, %2 offset:%3\n\ts_nop 1" ::"v"(voff), "v"(w), "s"(sbase), "n"(OFF) : "memory");
  else if (MODE == 1) asm volatile("global_store_dwordx4 %0, %1, %2 offset:%3 nt\n\ts_nop 1" ::"v"(voff), "v"(w), "s"(sbase), "n"(OFF) : "memory");
  else asm volatile("global_store_dwordx4 %0, %1, %2 offset:%3 sc1\n\ts_nop 1" ::"v"(voff), "v"(w), "s"(sbase), "n"(OFF) : "memory");
}

__global__ __launch_bounds__(512, 2) void k_gemm1(const u16* __restrict__ x, const u16* __restrict__ wqkv, const u16* __restrict__ wg,
                                                  const u16* __restrict__ wu, const u16* __restrict__ gate_w, char* __restrict__ ws,
                                                  const u16* __restrict__ wd, const u16* __restrict__ wo, const u16* outp) {
  extern __shared__ __attribute__((aligned(16))) char lds_[];
  char LAS* lds = (char LAS*)lds_;
  const unsigned lds0 = (unsigned)(uintptr_t)lds;
  const int tid = threadIdx.x, lane = tid & 63;
  const int wid = __builtin_amdgcn_readfirstlane(tid >> 6);
  const int wr = wid >> 2, wc = wid & 3;
  const int bid = blockIdx.x;
  const int xcd = bid & 7, idx = bid >> 3;
  const int sp = xcd >> 1, tm = (xcd & 1) * 4 + (idx >> 3), ct = idx & 7;
  const int tn = sp * 8 + ct;

  int ptype[5], pcol[5];
  {
    auto qc = [&](int i) { return (i >> 2) * 1024 + 256 * sp + 64 * (i & 3); };
    if (ct < 6) {
      ptype[0] = 1; ptype[1] = 2; pcol[0] = pcol[1] = 896 * sp + 128 * ct;
      ptype[2] = 1; ptype[3] = 2; pcol[2] = pcol[3] = 896 * sp + 128 * ct + 64;
      ptype[4] = 0; pcol[4] = qc(ct);
    } else {
      ptype[0] = 1; ptype[1] = 2; pcol[0] = pcol[1] = 896 * sp + 768 + 64 * (ct - 6);
      ptype[2] = ptype[3] = ptype[4] = 0;
      pcol[2] = qc(6 + 3 * (ct - 6)); pcol[3] = qc(7 + 3 * (ct - 6)); pcol[4] = qc(8 + 3 * (ct - 6));
    }
  }

  const char* sA = (const char*)x + (size_t)(tm * 256 + wid * 8) * 2048;
  unsigned voffA;
  { const int r = lane >> 3, j = lane & 7; voffA = r * 2048 + ((j ^ ((4 * (wid & 1) + (r >> 1)) & 7)) << 4); }
  const unsigned ldsA = lds0 + G1_LDS_A + wid * 1024;
  const int myp = wid >> 1;
  int mt = ptype[0], mc = pcol[0];
#pragma unroll
  for (int p = 1; p < 4; ++p) if (myp == p) { mt = ptype[p]; mc = pcol[p]; }
  const unsigned strB = (mt == 0) ? 6144u : 7168u;
  const char* sB = (const char*)(mt == 0 ? wqkv : (mt == 1 ? wg : wu)) + (size_t)mc * 2 + (size_t)(16 * (wid & 1)) * strB;
  unsigned voffB0, voffB1;
  { const int r = lane >> 3, j = lane & 7, q = r & 3;
    const int f0 = (q >> 1) & 1, f1 = f0 | 2;
    voffB0 = r * strB + ((((j >> 1) ^ f0) << 1 | (j & 1)) << 4);
    voffB1 = (r + 8) * strB + ((((j >> 1) ^ f1) << 1 | (j & 1)) << 4); }
  const unsigned ldsB = lds0 + G1_LDS_B + myp * 8192 + (wid & 1) * 2048;
  constexpr unsigned strB4 = 6144u;
  const char* sB4 = (const char*)wqkv + (size_t)pcol[4] * 2 + (size_t)(8 * wid) * strB4;
  unsigned voffB4;
  { const int r = lane >> 3, j = lane & 7, q = r & 3;
    const int f = ((q >> 1) & 1) | ((wid & 1) << 1);
    voffB4 = r * strB4 + ((((j >> 1) ^ f) << 1 | (j & 1)) << 4); }
  const unsigned ldsB4 = lds0 + G1_LDS_B4 + wid * 1024;

#define KC(kt) ((unsigned)((kt) < 15 ? (kt) : 15))
#define G1_A0(kt, b) do { const char* _p = sA + KC(kt) * 128u; glds16(_p, voffA, ldsA + (b) * 32768); \
                          glds16(_p + 64 * 2048, voffA, ldsA + (b) * 32768 + 8192); } while (0)
#define G1_A1(kt, b) do { const char* _p = sA + KC(kt) * 128u; glds16(_p + 128 * 2048, voffA, ldsA + (b) * 32768 + 16384); \
                          glds16(_p + 192 * 2048, voffA, ldsA + (b) * 32768 + 24576); } while (0)
#define G1_X(kt, b) do { const char* _p = sB + KC(kt) * 64u * strB; glds16(_p, voffB0, ldsB + (b) * 32768); \
                         glds16(_p, voffB1, ldsB + (b) * 32768 + 1024); } while (0)
#define G1_Y(kt, b) do { const char* _p = sB + (KC(kt) * 64u + 32u) * strB; glds16(_p, voffB0, ldsB + (b) * 32768 + 4096); \
                         glds16(_p, voffB1, ldsB + (b) * 32768 + 4096 + 1024); } while (0)
#define G1_Z(kt, b) glds16(sB4 + KC(kt) * 64u * strB4, voffB4, ldsB4 + (b) * 8192)

  STAMP_DECL;
  STAMP(0, 0);
  glds4((const char*)gate_w + wid * 256, lane * 4, lds0 + G1_LDS_GW + wid * 256);
  G1_X(0, 0); G1_Z(0, 0); G1_A0(0, 0); G1_A1(0, 0); G1_Y(0, 0);
#define TOUCH 1
#define TSTR 128
#define TD 4
  const char* tp; unsigned voffT, tstep, tlead;
  if (wid == 0) { tp = (const char*)x + (size_t)(tm * 256 + ct * 32) * 2048; voffT = (lane & 31) * 2048; tstep = 128; tlead = TD; }
  else if (wid <= 5) {
    int t_ = ptype[0], c_ = pcol[0];
#pragma unroll
    for (int p = 1; p < 5; ++p) if (wid - 1 == p) { t_ = ptype[p]; c_ = pcol[p]; }
    const unsigned st_ = (t_ == 0) ? 6144u : 7168u;
    tp = (const char*)(t_ == 0 ? wqkv : (t_ == 1 ? wg : wu)) + (size_t)c_ * 2 + (size_t)((tm & 3) * 16) * st_;
    voffT = (lane & 15) * st_; tstep = 64 * st_; tlead = TD;
  } else if (wid == 6) { tp = (const char*)wd + (size_t)bid * 28672; voffT = (lane % 14) * 128; tstep = 1792; tlead = 0; }
  else { tp = (const char*)wo + (size_t)bid * 8192; voffT = (lane & 3) * 128; tstep = 512; tlead = 0; }
  auto touch = [&](int j) { const unsigned k_ = (unsigned)j + tlead; glds4(tp + (k_ < 15u ? k_ : 15u) * tstep, voffT, lds0 + G1_LDS_DUMMY + wid * 256); };
  if (TOUCH) touch(tlead ? -1 : 0);
  G1_X(1, 1); G1_Z(1, 1);

  const int fr = lane & 15, g4 = lane >> 4;
  const char LAS* rA0 = lds + G1_LDS_A + wr * 8192 + fr * 128 + (((0 + g4) ^ (fr >> 1)) << 4);
  const char LAS* rA1 = lds + G1_LDS_A + wr * 8192 + fr * 128 + (((4 + g4) ^ (fr >> 1)) << 4);
  const char LAS* rB; const char LAS* rB4;
  { const int q = (lane & 15) >> 2, p = lane & 3;
    const int f = ((q >> 1) & 1) | ((g4 & 1) << 1);
    const int o = g4 * 1024 + q * 128 + ((wc ^ f) << 5) + p * 8;
    rB = lds + G1_LDS_B + o;
    rB4 = lds + G1_LDS_B4 + o; }

  const char LAS* rG; const char LAS* rGW;
  { const int r = tn * 8 + wid, l = lane & 31;
    rG = lds + G1_LDS_A + r * 128 + (((l >> 2) ^ ((r >> 1) & 7)) << 4) + (l & 3) * 4;
    rGW = lds + G1_LDS_GW + l * 4; }
  float gsum = 0.f;
  f32x4 acc[2][4][5];
#pragma unroll
  for (int a = 0; a < 2; ++a)
#pragma unroll
    for (int m = 0; m < 4; ++m)
#pragma unroll
      for (int p = 0; p < 5; ++p) acc[a][m][p] = (f32x4){0.f, 0.f, 0.f, 0.f};
  bf16x8 Af[4], Bf[5];

#define G1_LDB(b, ks) do { _Pragma("unroll") for (int p = 0; p < 4; ++p) { \
      s16x4 lo = lds_tr(rB + (b) * 32768 + p * 8192 + (ks) * 4096), hi = lds_tr(rB + (b) * 32768 + p * 8192 + (ks) * 4096 + 512); Bf[p] = cat8(lo, hi); } \
    { s16x4 lo = lds_tr(rB4 + (b) * 8192 + (ks) * 4096), hi = lds_tr(rB4 + (b) * 8192 + (ks) * 4096 + 512); Bf[4] = cat8(lo, hi); } } while (0)
#define G1_LDA(b, ai, ks) do { _Pragma("unroll") for (int m = 0; m < 4; ++m) \
      Af[m] = lds_b128(((ks) ? rA1 : rA0) + (b) * 32768 + (ai) * 16384 + m * 2048); } while (0)
#define G1_MMA(ai) do { __builtin_amdgcn_s_setprio(1); _Pragma("unroll") for (int m = 0; m < 4; ++m) _Pragma("unroll") for (int p = 0; p < 5; ++p) \
      acc[ai][m][p] = __builtin_amdgcn_mfma_f32_16x16x32_bf16(Bf[p], Af[m], acc[ai][m][p], 0, 0, 0); __builtin_amdgcn_s_setprio(0); } while (0)

#define WV_A WAIT_V(8)
#define WV_B WAIT_V(10)
  WV_A; BAR;
  STAMP(0, 1);
  if (wr == 1) BAR;

#define G1_TILE(t, b, I1, I2, W0, W1, W3) do { \
    G1_LDB(b, 0); G1_LDA(b, 0, 0); if (I1) G1_A0((t) + 1, (b) ^ 1); W0; BAR; WAIT_L0; G1_MMA(0); BAR; SCHED; \
    G1_LDA(b, 1, 0); if (I1) { G1_A1((t) + 1, (b) ^ 1); G1_Y((t) + 1, (b) ^ 1); } W1; BAR; WAIT_L0; G1_MMA(1); BAR; SCHED; \
    G1_LDB(b, 1); G1_LDA(b, 0, 1); \
    { const unsigned xa = *(const LAS unsigned*)(rG + (b) * 32768), wa = *(const LAS unsigned*)(rGW + (t) * 128); \
      if (TOUCH && (I2)) touch((t) + (tlead ? 0 : 1)); \
      BAR; WAIT_L0; \
      gsum = __builtin_fmaf(__uint_as_float(xa << 16), __uint_as_float(wa << 16), gsum); \
      gsum = __builtin_fmaf(__uint_as_float(xa & 0xffff0000u), __uint_as_float(wa & 0xffff0000u), gsum); } \
    G1_MMA(0); BAR; SCHED; \
    G1_LDA(b, 1, 1); if (I2) { G1_X((t) + 2, b); G1_Z((t) + 2, b); } W3; BAR; WAIT_L0; G1_MMA(1); BAR; SCHED; \
  } while (0)

#pragma unroll 1
  for (int t = 0; t < 16; t += 2) {
    G1_TILE(t, 0, 1, 1, WV_A, WV_B, WV_A);
    G1_TILE(t + 1, 1, 1, 1, WV_A, WV_B, WV_A);
  }
  if (wr == 0) BAR;
  STAMP(0, 2);
  const int lane_e = fresh_lane();
#define lane lane_e

  {
#pragma unroll
    for (int o = 16; o >= 1; o >>= 1) gsum += __shfl_xor(gsum, o);
    if (lane == 0) ((float*)(ws + WS_G))[tm * 256 + tn * 8 + wid] = 1.0f / (1.0f + __expf(-gsum));
    if (wid == 0 && lane == 0) ((unsigned*)(ws + WS_CNT))[(16 + bid) * 32] = 0u;
    if (wid == 1 && lane == 0 && bid < 16) ((unsigned*)(ws + WS_CNT))[bid * 32] = 0u;
  }

  const unsigned voffS = (((lane >> 4) & 1) * 16 + (lane & 15)) * 32 + (lane >> 5) * 16;
  const size_t rowoff = (size_t)(tm * 256 + wr * 64) * 32 + 2048;
#define G1_STQ(p) do { const float sc = pcol[p] < D_ ? QSCALE : 1.0f; \
    const char* dst = ws + WS_QKV + (size_t)(pcol[p] / 16 + wc) * (S_ * 32) + rowoff; \
    { const f32x4 u = acc[0][0][p] * sc, v = acc[0][1][p] * sc; store_pair16<QMODE, -2048>(dst, voffS, cvtpk(u[0], u[1]), cvtpk(u[2], u[3]), cvtpk(v[0], v[1]), cvtpk(v[2], v[3])); } \
    { const f32x4 u = acc[0][2][p] * sc, v = acc[0][3][p] * sc; store_pair16<QMODE, -1024>(dst, voffS, cvtpk(u[0], u[1]), cvtpk(u[2], u[3]), cvtpk(v[0], v[1]), cvtpk(v[2], v[3])); } \
    { const f32x4 u = acc[1][0][p] * sc, v = acc[1][1][p] * sc; store_pair16<QMODE, 2048>(dst, voffS, cvtpk(u[0], u[1]), cvtpk(u[2], u[3]), cvtpk(v[0], v[1]), cvtpk(v[2], v[3])); } \
    { const f32x4 u = acc[1][2][p] * sc, v = acc[1][3][p] * sc; store_pair16<QMODE, 3072>(dst, voffS, cvtpk(u[0], u[1]), cvtpk(u[2], u[3]), cvtpk(v[0], v[1]), cvtpk(v[2], v[3])); } } while (0)
#define G1_H1(pp, a, m, OFF) do { const f32x4 g0 = acc[a][m][2 * (pp)], u0 = acc[a][m][2 * (pp) + 1], g1 = acc[a][(m) + 1][2 * (pp)], u1 = acc[a][(m) + 1][2 * (pp) + 1]; \
    const f32x2 h0 = geglu2(g0.xy, u0.xy), h1 = geglu2(g0.zw, u0.zw), h2 = geglu2(g1.xy, u1.xy), h3 = geglu2(g1.zw, u1.zw); \
    store_pair16<HMODE, OFF>(dst, voffS, cvtpk(h0.x, h0.y), cvtpk(h1.x, h1.y), cvtpk(h2.x, h2.y), cvtpk(h3.x, h3.y)); } while (0)
#define G1_STH(pp) do { const char* dst = ws + WS_A2 + (size_t)(pcol[2 * (pp)] / 16 + wc) * (S_ * 32) + rowoff; \
    G1_H1(pp, 0, 0, -2048); G1_H1(pp, 0, 2, -1024); G1_H1(pp, 1, 0, 2048); G1_H1(pp, 1, 2, 3072); } while (0)
#define G1_TB(p) glds4(ws + WS_QKV + (size_t)(pcol[p] / 16 + wc) * (S_ * 32) + rowoff - 2048, voffTB, lds0 + G1_LDS_DUMMY + wid * 256)
  const unsigned voffTB = ((lane >> 4) & 1) * 4096 + (lane & 15) * 128;
  G1_STQ(4);
  if (ptype[2] == 0) {
    G1_STQ(2); G1_STQ(3);
    G1_STH(0);
    if (TBACK) { WAIT_V(4); G1_TB(4); G1_TB(2); G1_TB(3); }
  } else {
    G1_STH(0);
    if (TBACK) { WAIT_V(4); G1_TB(4); }
    G1_STH(1);
  }
#undef lane
  STAMP(0, 3);
  STAMP(0, 4);
  STAMP_END(0, 5);
}

constexpr int K2_SLOT = 24576;
constexpr int K2_G = 6 * K2_SLOT;
constexpr int K2_LDS = K2_G + 1024;
constexpr int AT_K = 3 * K2_SLOT;
constexpr int AT_V = AT_K + 32768;
constexpr int AT_V1 = AT_V + 16384 + 128;
static_assert(AT_V1 + 16384 <= K2_LDS, "lds");
constexpr int NH = 28, NC = 16, NS = NH + NC;

__device__ __forceinline__ void st16_sc1(void* p, u32x4 w) {
  asm volatile("global_store_dwordx4 %0, %1, off sc1\n\ts_nop 1" ::"v"(p), "v"(w) : "memory");
}
template <int SC1>
__device__ __forceinline__ void glds16x4(const void* sbase, unsigned voff, unsigned lds_addr) {
  if (SC1)
    asm volatile("s_mov_b32 m0, %2\n\ts_nop 0\n\tglobal_load_lds_dwordx4 %0, %1 sc1\n\tglobal_load_lds_dwordx4 %0, %1 offset:1024 sc1\n\t"
                 "global_load_lds_dwordx4 %0, %1 offset:2048 sc1\n\tglobal_load_lds_dwordx4 %0, %1 offset:3072 sc1" ::"v"(voff), "s"(sbase), "s"(lds_addr) : "memory");
  else
    asm volatile("s_mov_b32 m0, %2\n\ts_nop 0\n\tglobal_load_lds_dwordx4 %0, %1\n\tglobal_load_lds_dwordx4 %0, %1 offset:1024\n\t"
                 "global_load_lds_dwordx4 %0, %1 offset:2048\n\tglobal_load_lds_dwordx4 %0, %1 offset:3072" ::"v"(voff), "s"(sbase), "s"(lds_addr) : "memory");
}
__device__ __forceinline__ void glds16x2(const void* sbase, unsigned voff0, unsigned voff1m, unsigned lds_addr) {
  asm volatile("s_mov_b32 m0, %3\n\ts_nop 0\n\tglobal_load_lds_dwordx4 %0, %2\n\tglobal_load_lds_dwordx4 %1, %2 offset:1024" ::"v"(voff0), "v"(voff1m),
               "s"(sbase), "s"(lds_addr) : "memory");
}
#define LP(a) ((const char LAS*)(uintptr_t)(a))

template <bool FIRST, class VW>
__device__ __forceinline__ void attn_wave(const char LAS* lds, char* ws, int wid, int lane, int head, int qrow, const bf16x8 (&qf)[2], float gval, VW vwait) {
  const int r32 = lane & 31, hi = lane >> 5;
  const char LAS* kbase = lds + AT_K + wid * 1024 + r32 * 32 + ((hi ^ ((r32 >> 3) & 1)) << 4);
  const int kb0 = FIRST ? (8 - wid) : 0;
  f32x16 s[9];
  const f32x16 zero = {0.f, 0.f, 0.f, 0.f, 0.f, 0.f, 0.f, 0.f, 0.f, 0.f, 0.f, 0.f, 0.f, 0.f, 0.f, 0.f};
  bf16x8 kf[9][2];
#pragma unroll
  for (int kb = 0; kb < 3; ++kb) { kf[kb][0] = lds_b128(kbase + kb * 1024); kf[kb][1] = lds_b128(kbase + 16384 + kb * 1024); }
#pragma unroll
  for (int r = 0; r < 16; ++r) {
    const int c = (r & 3) + 8 * (r >> 2) + 4 * hi;
    s[0][r] = (c > r32) ? 0.f : -INFINITY;
    s[8][r] = (c > r32) ? -INFINITY : 0.f;
  }
  float ma = -INFINITY, mb = -INFINITY;
  SCHED;
#pragma unroll
  for (int kb = 0; kb < 9; ++kb) {
    if (kb + 3 < 9) { kf[kb + 3][0] = lds_b128(kbase + (kb + 3) * 1024); kf[kb + 3][1] = lds_b128(kbase + 16384 + (kb + 3) * 1024); }
    f32x16 z = (kb == 0 || kb == 8) ? s[kb] : zero;
    z = __builtin_amdgcn_mfma_f32_32x32x16_bf16(kf[kb][0], qf[0], z, 0, 0, 0);
    z = __builtin_amdgcn_mfma_f32_32x32x16_bf16(kf[kb][1], qf[1], z, 0, 0, 0);
    s[kb] = z;
    if (kb >= 2) {
      if (!FIRST || kb - 2 >= kb0) {
#pragma unroll
        for (int r = 0; r < 16; r += 4) {
          ma = __builtin_fmaxf(__builtin_fmaxf(ma, s[kb - 2][r]), s[kb - 2][r + 1]);
          mb = __builtin_fmaxf(__builtin_fmaxf(mb, s[kb - 2][r + 2]), s[kb - 2][r + 3]);
        }
      }
    }
    SCHED;
  }
#pragma unroll
  for (int kb = 7; kb < 9; ++kb)
    if (!FIRST || kb >= kb0) {
#pragma unroll
      for (int r = 0; r < 16; r += 4) {
        ma = __builtin_fmaxf(__builtin_fmaxf(ma, s[kb][r]), s[kb][r + 1]);
        mb = __builtin_fmaxf(__builtin_fmaxf(mb, s[kb][r + 2]), s[kb][r + 3]);
      }
    }
  float m = __builtin_fmaxf(ma, mb);
  { auto rr = __builtin_amdgcn_permlane32_swap(__float_as_uint(m), __float_as_uint(m), false, false);
    m = __builtin_fmaxf(__uint_as_float(rr[0]), __uint_as_float(rr[1])); }
  vwait();
  f32x16 o = zero;
  f32x4 lsum = {0.f, 0.f, 0.f, 0.f};
  bf16x8 sel;
  { const short one = (((lane >> 4) & 1) == (lane & 1)) ? (short)0x3F80 : (short)0;
    sel = (bf16x8){one, one, one, one, one, one, one, one}; }
  const char LAS* vbase;
  { const int g = lane >> 4, q = (lane & 15) >> 2, p = lane & 3;
    vbase = lds + ((g & 1) ? AT_V1 : AT_V) + wid * 1024 + (4 * (g >> 1) + q) * 32 + p * 8; }
  const f32x2 nm = {-m, -m};
  s16x4 vf[9][4];
#pragma unroll
  for (int i = 0; i < 4; ++i) vf[0][i] = lds_tr(vbase + i * 256);
#pragma unroll
  for (int kb = 0; kb < 9; ++kb) {
    if (kb + 1 < 9) {
#pragma unroll
      for (int i = 0; i < 4; ++i) vf[kb + 1][i] = lds_tr(vbase + (kb + 1) * 1024 + i * 256);
    }
    SCHED;
    if (!FIRST || kb >= kb0) {
      unsigned pk[8];
#pragma unroll
      for (int r = 0; r < 16; r += 2) {
        const f32x2 d = (f32x2){s[kb][r], s[kb][r + 1]} + nm;
        pk[r >> 1] = cvtpk(__builtin_amdgcn_exp2f(d.x), __builtin_amdgcn_exp2f(d.y));
      }
#pragma unroll
      for (int st = 0; st < 2; ++st) {
        u32x4 pw = {pk[4 * st], pk[4 * st + 1], pk[4 * st + 2], pk[4 * st + 3]};
        o = __builtin_amdgcn_mfma_f32_32x32x16_bf16(cat8(vf[kb][2 * st], vf[kb][2 * st + 1]), __builtin_bit_cast(bf16x8, pw), o, 0, 0, 0);
        lsum = __builtin_amdgcn_mfma_f32_16x16x32_bf16(sel, __builtin_bit_cast(bf16x8, pw), lsum, 0, 0, 0);
      }
    }
  }
  const float l = (lane & 16) ? lsum[1] : lsum[0];
  const float sc = gval * __builtin_amdgcn_rcpf(l);
  u16* a2 = (u16*)(ws + WS_A2);
#pragma unroll
  for (int dh = 0; dh < 2; ++dh) {
    const unsigned a0 = cvtpk(o[8 * dh + 0] * sc, o[8 * dh + 1] * sc), a1 = cvtpk(o[8 * dh + 2] * sc, o[8 * dh + 3] * sc);
    const unsigned b0 = cvtpk(o[8 * dh + 4] * sc, o[8 * dh + 5] * sc), b1 = cvtpk(o[8 * dh + 6] * sc, o[8 * dh + 7] * sc);
    auto r0 = __builtin_amdgcn_permlane32_swap(a0, b0, false, false);
    auto r1 = __builtin_amdgcn_permlane32_swap(a1, b1, false, false);
    u32x4 w = {r0[0], r1[0], r0[1], r1[1]};
    st16_sc1(a2 + ((size_t)(224 + head * 2 + dh) * S_ + qrow) * 16 + hi * 8, w);
  }
}

__device__ __forceinline__ void wait_count(unsigned* c, unsigned want) {
  unsigned spins = 0;
  while ((unsigned)__builtin_amdgcn_readfirstlane(__hip_atomic_load(c, __ATOMIC_RELAXED, __HIP_MEMORY_SCOPE_AGENT)) < want) {
    __builtin_amdgcn_s_sleep(1);
    if (++spins > (1u << 22)) break;
  }
  asm volatile("" ::: "memory");
}

__global__ __launch_bounds__(512, 2) void k2_fused(const u16* __restrict__ wd, const u16* __restrict__ wo, char* __restrict__ ws,
                                                   u16* __restrict__ out) {
  extern __shared__ __attribute__((aligned(16))) char lds_[];
  char LAS* lds = (char LAS*)lds_;
  const unsigned lds0 = (unsigned)(uintptr_t)lds;
  const int tid = threadIdx.x, lane = tid & 63;
  const int wid = __builtin_amdgcn_readfirstlane(tid >> 6);
  const int bid = blockIdx.x;
  const int xcd = bid & 7, idx = bid >> 3;
  const int sp = xcd >> 1, tn = idx & 7, tm = (xcd & 1) * 4 + (idx >> 3);
  const int mp = sp >> 1, np = sp & 1;
  const int tile = tm * 8 + tn;
  const int head = sp * 8 + tn, rb = tm;
  const int q0 = rb * 256;
  const char* qkv = ws + WS_QKV;
  unsigned* cntA = (unsigned*)(ws + WS_CNT);
  unsigned* cntP = (unsigned*)(ws + WS_CNT + 16 * 128);
  STAMP_DECL;
  STAMP(1, 0);

  const int qrow = q0 + wid * 32 + (lane & 31);
  bf16x8 qf[2];
  float gval;
  {
    const char* qp = qkv + ((size_t)(head * 2) * S_ + q0 + wid * 32) * 32;
    const unsigned voffQ = (lane & 31) * 32 + (lane >> 5) * 16;
    asm volatile("global_load_dwordx4 %0, %3, %4\n\tglobal_load_dwordx4 %1, %3, %5\n\tglobal_load_dword %2, %6, %7"
                 : "=&v"(qf[0]), "=&v"(qf[1]), "=&v"(gval)
                 : "v"(voffQ), "s"(qp), "s"(qp + S_ * 32), "v"((lane & 31) * 4), "s"(ws + WS_G + (q0 + wid * 32) * 4) : "memory");
    const unsigned voffK = (lane >> 1) * 32 + (((lane & 1) ^ ((lane >> 4) & 1)) << 4);
    const unsigned voffV = lane * 16;
    const int rowlo = (rb > 0 ? q0 - 256 : 0) + wid * 32, rowhi = q0 + wid * 32;
#pragma unroll
    for (int dh = 0; dh < 2; ++dh) {
      glds16(qkv + ((size_t)(64 + head * 2 + dh) * S_ + rowhi) * 32, voffK, lds0 + AT_K + dh * 16384 + (wid + 8) * 1024);
      glds16(qkv + ((size_t)(64 + head * 2 + dh) * S_ + rowlo) * 32, voffK, lds0 + AT_K + dh * 16384 + wid * 1024);
    }
#pragma unroll
    for (int dh = 0; dh < 2; ++dh) {
      glds16(qkv + ((size_t)(128 + head * 2 + dh) * S_ + rowhi) * 32, voffV, lds0 + (dh ? AT_V1 : AT_V) + (wid + 8) * 1024);
      glds16(qkv + ((size_t)(128 + head * 2 + dh) * S_ + rowlo) * 32, voffV, lds0 + (dh ? AT_V1 : AT_V) + wid * 1024);
    }
  }

  const int ld = wid & 3;
  unsigned voffA, voffB0, voffB1m;
  auto set_voff = [&]() {
    const int l_ = fresh_lane();
    const int r = l_ >> 3, j = l_ & 7, q = r & 3;
    const int f0 = (q >> 1) & 1, f1 = f0 | 2;
    voffA = l_ * 16;
    voffB0 = r * 2048 + ((((j >> 1) ^ f0) << 1 | (j & 1)) << 4);
    voffB1m = (r + 8) * 2048 + ((((j >> 1) ^ f1) << 1 | (j & 1)) << 4) - 1024;
  };
  const char* hA = ws + WS_A2 + ((size_t)(sp * NH * 2 + (ld >> 1)) * S_ + tm * 256 + ((ld & 1) ^ mp) * 128) * 32;
  const char* hB = (const char*)wd + (size_t)(sp * NH * 32 + 16 * (ld & 1)) * 2048 + (size_t)(tn * 128 + ((ld >> 1) ^ np) * 64) * 2;
  const char* cA = ws + WS_A2 + ((size_t)(224 + ld) * S_ + tm * 256 + mp * 128) * 32;
  const char* cB = (const char*)wo + (size_t)(16 * ld) * 2048 + (size_t)(tn * 128 + np * 64) * 2;
  auto issue = [&](int i) {
    const unsigned dst = lds0 + (i % 6) * K2_SLOT;
    if (i < NH) {
      glds16x4<0>(hA + (size_t)i * (2 * S_ * 32), voffA, dst + ld * 4096);
      glds16x2(hB + (size_t)i * (32 * 2048), voffB0, voffB1m, dst + 16384 + ld * 2048);
    } else {
      glds16x4<1>(cA + (size_t)(i - NH) * (4 * S_ * 32), voffA, dst + ld * 4096);
      glds16x2(cB + (size_t)(i - NH) * (64 * 2048), voffB0, voffB1m, dst + 16384 + ld * 2048);
    }
  };
  asm volatile("s_waitcnt vmcnt(4)" : "+v"(qf[0]), "+v"(qf[1]), "+v"(gval) :: "memory");
  BAR;
  asm volatile("" ::: "memory");
  STAMP(1, 1);
  if (wid >= 4) { set_voff(); issue(0); issue(1); issue(2); }
  if (lane < 32) *(LAS float*)(lds + K2_G + (wid * 32 + lane) * 4) = 1.0f - gval;

  auto vwait = [&]() {
    if (wid >= 4) { WAIT_V(18); } else { WAIT_V(0); }
    BAR;
    asm volatile("" ::: "memory");
  };
  if (rb == 0) attn_wave<true>(lds, ws, wid, lane, head, qrow, qf, gval, vwait);
  else attn_wave<false>(lds, ws, wid, lane, head, qrow, qf, gval, vwait);
  STAMP(1, 2);

  if (wid >= 4) {
    set_voff();
    WAIT_V(2);
    WAIT_L0; BAR;
    issue(3); issue(4);
    BAR;
    unsigned* ca = cntA + (tm * 2 + mp) * 32;
    unsigned fl = 0;
    for (int P = 0; P < NS / 2; ++P) {
      const int a = 2 * P + 5;
      if (P == 9) asm volatile("global_load_dword %0, %1, off sc1" : "=v"(fl) : "v"(ca) : "memory");
      if (P == 11) {
        asm volatile("" : "+v"(fl));
        if ((unsigned)__builtin_amdgcn_readfirstlane(fl) < 128u) wait_count(ca, 128u);
      }
      if (a + 1 < NS) { issue(a); issue(a + 1); WAIT_V(12); }
      else if (a < NS) { issue(a); WAIT_V(6); }
      else { WAIT_V(0); }
      if (P == 0 && voffA == 0) __hip_atomic_fetch_add(cntA + (rb * 2 + 1) * 32, 1u, __ATOMIC_RELAXED, __HIP_MEMORY_SCOPE_AGENT);
      if (P + 1 < NS / 2) BAR;
    }
    STAMP(1, 3); STAMP(1, 4); STAMP(1, 5); STAMP(1, 6);
    STAMP_END(1, 7);
    return;
  }

  const int wm = wid & 1, wn = wid >> 1;
  const int fr = lane & 15, g4 = lane >> 4;
  const unsigned rAh = lds0 + (g4 >> 1) * 8192 + (wm * 64 + fr) * 32 + (g4 & 1) * 16;
  unsigned rAc = 0;
  unsigned rB0, rB1;
  { const int q = (lane & 15) >> 2, p = lane & 3;
    const int f = ((q >> 1) & 1) | ((g4 & 1) << 1);
    rB0 = lds0 + 16384 + g4 * 1024 + q * 128 + (((wn * 2) ^ f) << 5) + p * 8;
    rB1 = rB0 ^ 32u; }

  f32x4 acc[8][4];
#pragma unroll
  for (int m = 0; m < 8; ++m)
#pragma unroll
    for (int n = 0; n < 4; ++n) acc[m][n] = (f32x4){0.f, 0.f, 0.f, 0.f};
  bf16x8 A0[8], A1[8];
  s16x4 L0[4], H0[4], L1[4], H1[4];
#define RD(AF, BL, BH, k, so, COV) do { \
    if ((k) < 8) { const int n_ = (k) >> 1; const unsigned b_ = ((n_ & 1) ? rB1 : rB0) + (so); \
      if ((k) & 1) BH[n_] = lds_tr(LP(b_) + (n_ >> 1) * 4096 + 512); else BL[n_] = lds_tr(LP(b_) + (n_ >> 1) * 4096); } \
    else { const int m_ = (k) - 8; \
      if (COV) AF[m_] = lds_b128(LP(rAc + (so)) + (m_ >> 2) * 8192 + (m_ & 3) * 512); \
      else AF[m_] = lds_b128(LP(rAh + (so)) + (m_ >> 2) * 4096 + (m_ & 3) * 512); } } while (0)
#define MFH(AF, BL, BH, k) acc[(k) >> 2][(k) & 3] = __builtin_amdgcn_mfma_f32_16x16x32_bf16(cat8(BL[(k) & 3], BH[(k) & 3]), AF[(k) >> 2], acc[(k) >> 2][(k) & 3], 0, 0, 0)
#define STAGE_H(AC, LC, HC, AN, LN, HN, so, COV) do { \
    _Pragma("unroll") for (int k = 0; k < 32; ++k) { MFH(AC, LC, HC, k); if (k >= 1 && k < 17) RD(AN, LN, HN, k - 1, so, COV); SCHED; } } while (0)

  WAIT_L0; BAR;
  asm volatile("" ::: "memory");
#pragma unroll
  for (int k = 0; k < 16; ++k) RD(A0, L0, H0, k, 0, 0);
  WAIT_L0; BAR;
  SCHED;
  STAMP(1, 3);
  int s1 = 1;
  for (int P = 0; P < NH / 2 - 1; ++P) {
    const int s2 = (s1 == 5) ? 0 : s1 + 1;
    STAGE_H(A0, L0, H0, A1, L1, H1, s1 * K2_SLOT, 0);
    STAGE_H(A1, L1, H1, A0, L0, H0, s2 * K2_SLOT, 0);
    s1 = s2 + 1;
    if (P == 2) { WAIT_V(0); if (lane == 0) __hip_atomic_fetch_add(cntA + (rb * 2) * 32, 1u, __ATOMIC_RELAXED, __HIP_MEMORY_SCOPE_AGENT); }
    WAIT_L0; BAR; SCHED;
  }
  STAGE_H(A0, L0, H0, A1, L1, H1, 3 * K2_SLOT, 0);
  { const int l2 = fresh_lane(); rAc = lds0 + (l2 >> 5) * 4096 + (wm * 64 + (l2 & 15)) * 32 + ((l2 >> 4) & 1) * 16; }
  STAGE_H(A1, L1, H1, A0, L0, H0, 4 * K2_SLOT, 1);
  WAIT_L0; BAR; SCHED;
  STAMP(1, 4);

  auto rs = __builtin_amdgcn_make_buffer_rsrc((void*)(ws + WS_PART), 0, 16u << 20, 0x00020000);
  float omg[8];
  const int l3 = fresh_lane();
#pragma unroll
  for (int m = 0; m < 8; ++m) omg[m] = *(const LAS float*)(lds + K2_G + (((m >> 2) ^ mp) * 128 + wm * 64 + (m & 3) * 16 + (l3 & 15)) * 4);
#pragma unroll
  for (int d = 3; d >= 1; --d) {
    const int h = d >> 1, c = d & 1;
#pragma unroll
    for (int mb = 0; mb < 4; ++mb) {
      const float sc = omg[4 * h + mb];
      const f32x4 u = acc[4 * h + mb][2 * c] * sc, v = acc[4 * h + mb][2 * c + 1] * sc;
      u32x4 w = {cvtpk(u[0], u[1]), cvtpk(u[2], u[3]), cvtpk(v[0], v[1]), cvtpk(v[2], v[3])};
      const unsigned off = (((((unsigned)tile * 4 + (d ^ sp)) * 4 + sp) * 4 + wid) * 4 + mb) * 1024u + l3 * 16;
      __builtin_amdgcn_raw_buffer_store_b128(w, rs, off, 0, 16);
    }
  }
  f32x4 acK[4][2];
#pragma unroll
  for (int mb = 0; mb < 4; ++mb)
#pragma unroll
    for (int j = 0; j < 2; ++j) acK[mb][j] = acc[mb][j] * omg[mb];
  SCHED;
#define MFC(AF, BL, BH, k) acK[((k) >> 1) & 3][(k) & 1] = __builtin_amdgcn_mfma_f32_16x16x32_bf16(cat8(BL[2 * ((k) >> 3) + ((k) & 1)], BH[2 * ((k) >> 3) + ((k) & 1)]), \
                                                             AF[4 * ((k) >> 3) + (((k) >> 1) & 3)], acK[((k) >> 1) & 3][(k) & 1], 0, 0, 0)
#define STAGE_C(AC, LC, HC, AN, LN, HN, so) do { \
    _Pragma("unroll") for (int k = 0; k < 16; ++k) { MFC(AC, LC, HC, k); RD(AN, LN, HN, k, so, 1); SCHED; } } while (0)
  u32x4 part[3][4];
  unsigned* myc = cntP + (tile * 4 + wid) * 32;
  unsigned flagv = 0;
#pragma unroll
  for (int P = NH / 2; P < NS / 2; ++P) {
    STAGE_C(A0, L0, H0, A1, L1, H1, ((2 * P + 1) % 6) * K2_SLOT);
    if (P + 1 < NS / 2) { STAGE_C(A1, L1, H1, A0, L0, H0, ((2 * P + 2) % 6) * K2_SLOT); }
    else {
#pragma unroll
      for (int k = 0; k < 16; ++k) MFC(A1, L1, H1, k);
    }
    if (P == NH / 2 + 2) { WAIT_V(0); if (l3 == 0) __hip_atomic_fetch_add(myc, 1u, __ATOMIC_RELAXED, __HIP_MEMORY_SCOPE_AGENT); }
    if (P == NS / 2 - 4) flagv = __hip_atomic_load(myc, __ATOMIC_RELAXED, __HIP_MEMORY_SCOPE_AGENT);
    if (P == NS / 2 - 3) {
      if ((unsigned)__builtin_amdgcn_readfirstlane(flagv) < 4u) wait_count(myc, 4u);
      asm volatile("" ::: "memory");
#pragma unroll
      for (int jj = 0; jj < 3; ++jj) {
        const int src = (sp + 1 + jj) & 3;
#pragma unroll
        for (int mb = 0; mb < 4; ++mb) {
          const unsigned off = (((((unsigned)tile * 4 + sp) * 4 + src) * 4 + wid) * 4 + mb) * 1024u + l3 * 16;
          part[jj][mb] = __builtin_amdgcn_raw_buffer_load_b128(rs, off, 0, 16);
        }
      }
    }
    if (P + 1 < NS / 2) { WAIT_L0; BAR; SCHED; }
  }
  STAMP(1, 5);
  const int lane_e = fresh_lane();
#pragma unroll
  for (int mb = 0; mb < 4; ++mb) {
    f32x4 u = acK[mb][0], v = acK[mb][1];
#pragma unroll
    for (int jj = 0; jj < 3; ++jj) {
      const u32x4 w = part[jj][mb];
      u[0] += __uint_as_float(w[0] << 16); u[1] += __uint_as_float(w[0] & 0xffff0000u);
      u[2] += __uint_as_float(w[1] << 16); u[3] += __uint_as_float(w[1] & 0xffff0000u);
      v[0] += __uint_as_float(w[2] << 16); v[1] += __uint_as_float(w[2] & 0xffff0000u);
      v[2] += __uint_as_float(w[3] << 16); v[3] += __uint_as_float(w[3] & 0xffff0000u);
    }
    auto r0 = __builtin_amdgcn_permlane16_swap(cvtpk(u[0], u[1]), cvtpk(v[0], v[1]), false, false);
    auto r1 = __builtin_amdgcn_permlane16_swap(cvtpk(u[2], u[3]), cvtpk(v[2], v[3]), false, false);
    u32x4 w = {r0[0], r1[0], r0[1], r1[1]};
    const int row = tm * 256 + mp * 128 + wm * 64 + mb * 16 + (lane_e & 15);
    const int col = tn * 128 + np * 64 + wn * 32 + ((lane_e >> 4) & 1) * 16 + (lane_e >> 5) * 8;
    *(u32x4*)(out + (size_t)row * D_ + col) = w;
  }
  STAMP(1, 6);
  STAMP_END(1, 7);
}

extern "C" void kernel_launch(void* const* d_in, const int* in_sizes, int n_in, void* d_out, int out_size, void* d_ws, size_t ws_size,
                              hipStream_t stream) {
  const u16* x = (const u16*)d_in[0];
  const u16* wqkv = (const u16*)d_in[1];
  const u16* wo = (const u16*)d_in[2];
  const u16* wg = (const u16*)d_in[4];
  const u16* wu = (const u16*)d_in[5];
  const u16* wd = (const u16*)d_in[6];
  const u16* gate_w = (const u16*)d_in[7];
  char* ws = (char*)d_ws;
  (void)hipFuncSetAttribute((const void*)k_gemm1, hipFuncAttributeMaxDynamicSharedMemorySize, G1_LDS);
  (void)hipFuncSetAttribute((const void*)k2_fused, hipFuncAttributeMaxDynamicSharedMemorySize, K2_LDS);
  k_gemm1<<<256, 512, G1_LDS, stream>>>(x, wqkv, wg, wu, gate_w, ws, wd, wo, (const u16*)d_out);
  k2_fused<<<256, 512, K2_LDS, stream>>>(wd, wo, ws, (u16*)d_out);
}
```

```cpp
#include <hip/hip_runtime.h>
#include <hip/hip_bf16.h>
#include <stdint.h>

#define LAS __attribute__((address_space(3)))
typedef unsigned short u16;
typedef short bf16x8 __attribute__((ext_vector_type(8)));
typedef short s16x4 __attribute__((ext_vector_type(4)));
typedef float f32x4 __attribute__((ext_vector_type(4)));
typedef float f32x16 __attribute__((ext_vector_type(16)));
typedef unsigned u32x4 __attribute__((ext_vector_type(4)));
typedef unsigned u32x2 __attribute__((ext_vector_type(2)));

constexpr int S_ = 2048, D_ = 1024, F_ = 3584, NQKV = 3072, K2 = F_ + D_;
constexpr float QSCALE = 0.17677669529663687f * 1.4426950408889634f;

constexpr size_t WS_QKV = 0;
constexpr size_t WS_A2 = WS_QKV + (size_t)NQKV * S_ * 2;
constexpr size_t WS_G = WS_A2 + (size_t)K2 * S_ * 2;
constexpr size_t WS_CNT = WS_G + S_ * 4;
constexpr size_t WS_PART = WS_CNT + 65536;
constexpr size_t WS_TOUCH_END = (WS_PART + (16u << 20) + 32767) / 32768 * 32768;

#define WAIT_V(n) asm volatile("s_waitcnt vmcnt(" #n ")" ::: "memory")
#define WAIT_L0 asm volatile("s_waitcnt lgkmcnt(0)" ::: "memory")
#define BAR __builtin_amdgcn_s_barrier()
#define SCHED __builtin_amdgcn_sched_barrier(0)

#define STAMP_DECL do {} while (0)
#define STAMP(kid, i) do {} while (0)
#define STAMP_END(kid, n) do {} while (0)
__device__ __forceinline__ int fresh_lane() {
  int l;
  asm volatile("v_mbcnt_lo_u32_b32 %0, -1, 0\n\tv_mbcnt_hi_u32_b32 %0, -1, %0" : "=v"(l));
  return l;
}
__device__ __forceinline__ void glds16(const void* sbase, unsigned voff, unsigned lds_addr) {
  asm volatile("s_mov_b32 m0, %2\n\ts_nop 0\n\tglobal_load_lds_dwordx4 %0, %1" ::"v"(voff), "s"(sbase), "s"(lds_addr) : "memory");
}
typedef __bf16 bf16x2_t __attribute__((ext_vector_type(2)));
typedef float f32x2 __attribute__((ext_vector_type(2)));
__device__ __forceinline__ unsigned cvtpk(float lo, float hi) {
  return __builtin_bit_cast(unsigned, __builtin_convertvector((f32x2){lo, hi}, bf16x2_t));
}
__device__ __forceinline__ bf16x8 lds_b128(const char LAS* p) { return *(const LAS bf16x8*)p; }
__device__ __forceinline__ s16x4 lds_tr(const char LAS* p) {
  return __builtin_bit_cast(s16x4, __builtin_amdgcn_ds_read_tr16_b64_v4i16((LAS s16x4*)p));
}
__device__ __forceinline__ bf16x8 cat8(s16x4 a, s16x4 b) {
  return (bf16x8){a[0], a[1], a[2], a[3], b[0], b[1], b[2], b[3]};
}
__device__ __forceinline__ float bf2f(unsigned short v) { return __uint_as_float((unsigned)v << 16); }
__device__ __forceinline__ float gelu_t(float x) {
  const float c0 = -2.0f * 0.7978845608028654f * 1.4426950408889634f;
  const float c1 = c0 * 0.044715f;
  float t = x * x;
  float z = x * __builtin_fmaf(t, c1, c0);
  float e = __builtin_amdgcn_exp2f(z);
  return x * __builtin_amdgcn_rcpf(1.0f + e);
}

__device__ __forceinline__ f32x2 geglu2(f32x2 x, f32x2 u) {
  const float c0 = -2.0f * 0.7978845608028654f * 1.4426950408889634f;
  const float c1 = c0 * 0.044715f;
  const f32x2 t = x * x;
  const f32x2 z = x * (t * c1 + c0);
  f32x2 e; e.x = __builtin_amdgcn_exp2f(z.x); e.y = __builtin_amdgcn_exp2f(z.y);
  const f32x2 d = e + 1.0f;
  f32x2 r; r.x = __builtin_amdgcn_rcpf(d.x); r.y = __builtin_amdgcn_rcpf(d.y);
  return (x * u) * r;
}
__device__ __forceinline__ void glds4(const void* sbase, unsigned voff, unsigned lds_addr) {
  asm volatile("s_mov_b32 m0, %2\n\ts_nop 0\n\tglobal_load_lds_dword %0, %1" ::"v"(voff), "s"(sbase), "s"(lds_addr) : "memory");
}

constexpr int G1_LDS_A = 0;
constexpr int G1_LDS_B = 65536;
constexpr int G1_LDS_B4 = 131072;
constexpr int G1_LDS_GW = 147456;
constexpr int G1_LDS_DUMMY = G1_LDS_GW + 2048;
constexpr int G1_LDS = G1_LDS_DUMMY + 2048;

#define QMODE 2
#define HMODE 2
#define TBACK 0
template <int MODE, int OFF>
__device__ __forceinline__ void store_pair16(const void* sbase, unsigned voff, unsigned x0, unsigned x1, unsigned y0, unsigned y1) {
  auto r0 = __builtin_amdgcn_permlane16_swap(x0, y0, false, false);
  auto r1 = __builtin_amdgcn_permlane16_swap(x1, y1, false, false);
  u32x4 w = {r0[0], r1[0], r0[1], r1[1]};
  if (MODE == 0) asm volatile("global_store_dwordx4 %0, %1, %2 offset:%3\n\ts_nop 1" ::"v"(voff), "v"(w), "s"(sbase), "n"(OFF) : "memory");
  else if (MODE == 1) asm volatile("global_store_dwordx4 %0, %1, %2 offset:%3 nt\n\ts_nop 1" ::"v"(voff), "v"(w), "s"(sbase), "n"(OFF) : "memory");
  else asm volatile("global_store_dwordx4 %0, %1, %2 offset:%3 sc1\n\ts_nop 1" ::"v"(voff), "v"(w), "s"(sbase), "n"(OFF) : "memory");
}

__global__ __launch_bounds__(512, 2) void k_gemm1(const u16* __restrict__ x, const u16* __restrict__ wqkv, const u16* __restrict__ wg,
                                                  const u16* __restrict__ wu, const u16* __restrict__ gate_w, char* __restrict__ ws,
                                                  const u16* __restrict__ wd, const u16* __restrict__ wo, const u16* outp) {
  extern __shared__ __attribute__((aligned(16))) char lds_[];
  char LAS* lds = (char LAS*)lds_;
  const unsigned lds0 = (unsigned)(uintptr_t)lds;
  const int tid = threadIdx.x, lane = tid & 63;
  const int wid = __builtin_amdgcn_readfirstlane(tid >> 6);
  const int wr = wid >> 2, wc = wid & 3;
  const int bid = blockIdx.x;
  const int xcd = bid & 7, idx = bid >> 3;
  const int sp = xcd >> 1, tm = (xcd & 1) * 4 + (idx >> 3), ct = idx & 7;
  const int tn = sp * 8 + ct;

  int ptype[5], pcol[5];
  {
    auto qc = [&](int i) { return (i >> 2) * 1024 + 256 * sp + 64 * (i & 3); };
    if (ct < 6) {
      ptype[0] = 1; ptype[1] = 2; pcol[0] = pcol[1] = 896 * sp + 128 * ct;
      ptype[2] = 1; ptype[3] = 2; pcol[2] = pcol[3] = 896 * sp + 128 * ct + 64;
      ptype[4] = 0; pcol[4] = qc(ct);
    } else {
      ptype[0] = 1; ptype[1] = 2; pcol[0] = pcol[1] = 896 * sp + 768 + 64 * (ct - 6);
      ptype[2] = ptype[3] = ptype[4] = 0;
      pcol[2] = qc(6 + 3 * (ct - 6)); pcol[3] = qc(7 + 3 * (ct - 6)); pcol[4] = qc(8 + 3 * (ct - 6));
    }
  }

  const char* sA = (const char*)x + (size_t)(tm * 256 + wid * 8) * 2048;
  unsigned voffA;
  { const int r = lane >> 3, j = lane & 7; voffA = r * 2048 + ((j ^ ((4 * (wid & 1) + (r >> 1)) & 7)) << 4); }
  const unsigned ldsA = lds0 + G1_LDS_A + wid * 1024;
  const int myp = wid >> 1;
  int mt = ptype[0], mc = pcol[0];
#pragma unroll
  for (int p = 1; p < 4; ++p) if (myp == p) { mt = ptype[p]; mc = pcol[p]; }
  const unsigned strB = (mt == 0) ? 6144u : 7168u;
  const char* sB = (const char*)(mt == 0 ? wqkv : (mt == 1 ? wg : wu)) + (size_t)mc * 2 + (size_t)(16 * (wid & 1)) * strB;
  unsigned voffB0, voffB1;
  { const int r = lane >> 3, j = lane & 7, q = r & 3;
    const int f0 = (q >> 1) & 1, f1 = f0 | 2;
    voffB0 = r * strB + ((((j >> 1) ^ f0) << 1 | (j & 1)) << 4);
    voffB1 = (r + 8) * strB + ((((j >> 1) ^ f1) << 1 | (j & 1)) << 4); }
  const unsigned ldsB = lds0 + G1_LDS_B + myp * 8192 + (wid & 1) * 2048;
  constexpr unsigned strB4 = 6144u;
  const char* sB4 = (const char*)wqkv + (size_t)pcol[4] * 2 + (size_t)(8 * wid) * strB4;
  unsigned voffB4;
  { const int r = lane >> 3, j = lane & 7, q = r & 3;
    const int f = ((q >> 1) & 1) | ((wid & 1) << 1);
    voffB4 = r * strB4 + ((((j >> 1) ^ f) << 1 | (j & 1)) << 4); }
  const unsigned ldsB4 = lds0 + G1_LDS_B4 + wid * 1024;

#define KC(kt) ((unsigned)((kt) < 15 ? (kt) : 15))
#define G1_A0(kt, b) do { const char* _p = sA + KC(kt) * 128u; glds16(_p, voffA, ldsA + (b) * 32768); \
                          glds16(_p + 64 * 2048, voffA, ldsA + (b) * 32768 + 8192); } while (0)
#define G1_A1(kt, b) do { const char* _p = sA + KC(kt) * 128u; glds16(_p + 128 * 2048, voffA, ldsA + (b) * 32768 + 16384); \
                          glds16(_p + 192 * 2048, voffA, ldsA + (b) * 32768 + 24576); } while (0)
#define G1_X(kt, b) do { const char* _p = sB + KC(kt) * 64u * strB; glds16(_p, voffB0, ldsB + (b) * 32768); \
                         glds16(_p, voffB1, ldsB + (b) * 32768 + 1024); } while (0)
#define G1_Y(kt, b) do { const char* _p = sB + (KC(kt) * 64u + 32u) * strB; glds16(_p, voffB0, ldsB + (b) * 32768 + 4096); \
                         glds16(_p, voffB1, ldsB + (b) * 32768 + 4096 + 1024); } while (0)
#define G1_Z(kt, b) glds16(sB4 + KC(kt) * 64u * strB4, voffB4, ldsB4 + (b) * 8192)

  STAMP_DECL;
  STAMP(0, 0);
  glds4((const char*)gate_w + wid * 256, lane * 4, lds0 + G1_LDS_GW + wid * 256);
  G1_X(0, 0); G1_A0(0, 0); G1_A1(0, 0); G1_Z(0, 0); G1_Y(0, 0); G1_X(1, 1);
#define TOUCH 1
#define TSTR 128
#define TD 4
  const char* tp; unsigned voffT, tstep, tlead;
  if (wid == 0) { tp = (const char*)x + (size_t)(tm * 256 + ct * 32) * 2048; voffT = (lane & 31) * 2048; tstep = 128; tlead = TD; }
  else if (wid <= 5) {
    int t_ = ptype[0], c_ = pcol[0];
#pragma unroll
    for (int p = 1; p < 5; ++p) if (wid - 1 == p) { t_ = ptype[p]; c_ = pcol[p]; }
    const unsigned st_ = (t_ == 0) ? 6144u : 7168u;
    tp = (const char*)(t_ == 0 ? wqkv : (t_ == 1 ? wg : wu)) + (size_t)c_ * 2 + (size_t)((tm & 3) * 16) * st_;
    voffT = (lane & 15) * st_; tstep = 64 * st_; tlead = TD;
  } else if (wid == 6) { tp = (const char*)wd + (size_t)bid * 28672; voffT = (lane % 14) * 128; tstep = 1792; tlead = 0; }
  else { tp = (const char*)wo + (size_t)bid * 8192; voffT = (lane & 3) * 128; tstep = 512; tlead = 0; }
  auto touch = [&](int j) { const unsigned k_ = (unsigned)j + tlead; glds4(tp + (k_ < 15u ? k_ : 15u) * tstep, voffT, lds0 + G1_LDS_DUMMY + wid * 256); };
  if (TOUCH) touch(tlead ? -1 : 0);

  const int fr = lane & 15, g4 = lane >> 4;
  const char LAS* rA0 = lds + G1_LDS_A + wr * 8192 + fr * 128 + (((0 + g4) ^ (fr >> 1)) << 4);
  const char LAS* rA1 = lds + G1_LDS_A + wr * 8192 + fr * 128 + (((4 + g4) ^ (fr >> 1)) << 4);
  const char LAS* rB; const char LAS* rB4;
  { const int q = (lane & 15) >> 2, p = lane & 3;
    const int f = ((q >> 1) & 1) | ((g4 & 1) << 1);
    const int o = g4 * 1024 + q * 128 + ((wc ^ f) << 5) + p * 8;
    rB = lds + G1_LDS_B + o;
    rB4 = lds + G1_LDS_B4 + o; }

  const char LAS* rG; const char LAS* rGW;
  { const int r = tn * 8 + wid, l = lane & 31;
    rG = lds + G1_LDS_A + r * 128 + (((l >> 2) ^ ((r >> 1) & 7)) << 4) + (l & 3) * 4;
    rGW = lds + G1_LDS_GW + l * 4; }
  float gsum = 0.f;
  f32x4 acc[2][4][5];
#pragma unroll
  for (int a = 0; a < 2; ++a)
#pragma unroll
    for (int m = 0; m < 4; ++m)
#pragma unroll
      for (int p = 0; p < 5; ++p) acc[a][m][p] = (f32x4){0.f, 0.f, 0.f, 0.f};
  bf16x8 Af[8], Bf[5];

#define G1_LDB(b, ks) do { _Pragma("unroll") for (int p = 0; p < 4; ++p) { \
      s16x4 lo = lds_tr(rB + (b) * 32768 + p * 8192 + (ks) * 4096), hi = lds_tr(rB + (b) * 32768 + p * 8192 + (ks) * 4096 + 512); Bf[p] = cat8(lo, hi); } \
    { s16x4 lo = lds_tr(rB4 + (b) * 8192 + (ks) * 4096), hi = lds_tr(rB4 + (b) * 8192 + (ks) * 4096 + 512); Bf[4] = cat8(lo, hi); } } while (0)
#define G1_LDA(b, ks) do { _Pragma("unroll") for (int m = 0; m < 8; ++m) \
      Af[m] = lds_b128(((ks) ? rA1 : rA0) + (b) * 32768 + (m >> 2) * 16384 + (m & 3) * 2048); } while (0)
#define EARLYB 0
#define G1_MMA() do { __builtin_amdgcn_s_setprio(1); _Pragma("unroll") for (int k = 0; k < 40; ++k) { const int m = k / 5, p = k % 5; \
      if (k == 40 - EARLYB) { SCHED; __builtin_amdgcn_s_setprio(3); BAR; SCHED; } \
      acc[m >> 2][m & 3][p] = __builtin_amdgcn_mfma_f32_16x16x32_bf16(Bf[p], Af[m], acc[m >> 2][m & 3][p], 0, 0, 0); } \
      __builtin_amdgcn_s_setprio(0); if (EARLYB == 0) BAR; } while (0)
#define WV_A WAIT_V(5)
#define WV_B WAIT_V(10)
  WV_A; BAR;
  STAMP(0, 1);
  if (wr == 1) BAR;

#define G1_TILE(t, b) do { \
    G1_LDB(b, 0); G1_LDA(b, 0); \
    G1_A0((t) + 1, (b) ^ 1); G1_A1((t) + 1, (b) ^ 1); G1_Z((t) + 1, (b) ^ 1); G1_Y((t) + 1, (b) ^ 1); \
    WV_B; WAIT_L0; BAR; G1_MMA(); SCHED; \
    G1_LDB(b, 1); G1_LDA(b, 1); \
    { const unsigned xa = *(const LAS unsigned*)(rG + (b) * 32768), wa = *(const LAS unsigned*)(rGW + (t) * 128); \
      G1_X((t) + 2, b); if (TOUCH) touch((t) + (tlead ? 0 : 1)); \
      WAIT_L0; \
      gsum = __builtin_fmaf(__uint_as_float(xa << 16), __uint_as_float(wa << 16), gsum); \
      gsum = __builtin_fmaf(__uint_as_float(xa & 0xffff0000u), __uint_as_float(wa & 0xffff0000u), gsum); \
      asm volatile("" : "+v"(gsum)); } \
    WV_A; BAR; G1_MMA(); SCHED; \
  } while (0)

#pragma unroll 1
  for (int t = 0; t < 16; t += 2) {
    G1_TILE(t, 0);
    G1_TILE(t + 1, 1);
  }
  if (wr == 0) BAR;
  STAMP(0, 2);
  const int lane_e = fresh_lane();
#define lane lane_e

  {
#pragma unroll
    for (int o = 16; o >= 1; o >>= 1) gsum += __shfl_xor(gsum, o);
    if (lane == 0) ((float*)(ws + WS_G))[tm * 256 + tn * 8 + wid] = 1.0f / (1.0f + __expf(-gsum));
    if (wid == 0 && lane == 0) ((unsigned*)(ws + WS_CNT))[(16 + bid) * 32] = 0u;
    if (wid == 1 && lane == 0 && bid < 16) ((unsigned*)(ws + WS_CNT))[bid * 32] = 0u;
  }

  const unsigned voffS = (((lane >> 4) & 1) * 16 + (lane & 15)) * 32 + (lane >> 5) * 16;
  const size_t rowoff = (size_t)(tm * 256 + wr * 64) * 32 + 2048;
#define G1_STQ(p) do { const float sc = pcol[p] < D_ ? QSCALE : 1.0f; \
    const char* dst = ws + WS_QKV + (size_t)(pcol[p] / 16 + wc) * (S_ * 32) + rowoff; \
    { const f32x4 u = acc[0][0][p] * sc, v = acc[0][1][p] * sc; store_pair16<QMODE, -2048>(dst, voffS, cvtpk(u[0], u[1]), cvtpk(u[2], u[3]), cvtpk(v[0], v[1]), cvtpk(v[2], v[3])); } \
    { const f32x4 u = acc[0][2][p] * sc, v = acc[0][3][p] * sc; store_pair16<QMODE, -1024>(dst, voffS, cvtpk(u[0], u[1]), cvtpk(u[2], u[3]), cvtpk(v[0], v[1]), cvtpk(v[2], v[3])); } \
    { const f32x4 u = acc[1][0][p] * sc, v = acc[1][1][p] * sc; store_pair16<QMODE, 2048>(dst, voffS, cvtpk(u[0], u[1]), cvtpk(u[2], u[3]), cvtpk(v[0], v[1]), cvtpk(v[2], v[3])); } \
    { const f32x4 u = acc[1][2][p] * sc, v = acc[1][3][p] * sc; store_pair16<QMODE, 3072>(dst, voffS, cvtpk(u[0], u[1]), cvtpk(u[2], u[3]), cvtpk(v[0], v[1]), cvtpk(v[2], v[3])); } } while (0)
#define G1_H1(pp, a, m, OFF) do { const f32x4 g0 = acc[a][m][2 * (pp)], u0 = acc[a][m][2 * (pp) + 1], g1 = acc[a][(m) + 1][2 * (pp)], u1 = acc[a][(m) + 1][2 * (pp) + 1]; \
    const f32x2 h0 = geglu2(g0.xy, u0.xy), h1 = geglu2(g0.zw, u0.zw), h2 = geglu2(g1.xy, u1.xy), h3 = geglu2(g1.zw, u1.zw); \
    store_pair16<HMODE, OFF>(dst, voffS, cvtpk(h0.x, h0.y), cvtpk(h1.x, h1.y), cvtpk(h2.x, h2.y), cvtpk(h3.x, h3.y)); } while (0)
#define G1_STH(pp) do { const char* dst = ws + WS_A2 + (size_t)(pcol[2 * (pp)] / 16 + wc) * (S_ * 32) + rowoff; \
    G1_H1(pp, 0, 0, -2048); G1_H1(pp, 0, 2, -1024); G1_H1(pp, 1, 0, 2048); G1_H1(pp, 1, 2, 3072); } while (0)
#define G1_TB(p) glds4(ws + WS_QKV + (size_t)(pcol[p] / 16 + wc) * (S_ * 32) + rowoff - 2048, voffTB, lds0 + G1_LDS_DUMMY + wid * 256)
  const unsigned voffTB = ((lane >> 4) & 1) * 4096 + (lane & 15) * 128;
  G1_STQ(4);
  if (ptype[2] == 0) {
    G1_STQ(2); G1_STQ(3);
    G1_STH(0);
    if (TBACK) { WAIT_V(4); G1_TB(4); G1_TB(2); G1_TB(3); }
  } else {
    G1_STH(0);
    if (TBACK) { WAIT_V(4); G1_TB(4); }
    G1_STH(1);
  }
#undef lane
  STAMP(0, 3);
  STAMP(0, 4);
  STAMP_END(0, 5);
}

constexpr int K2_SLOT = 24576;
constexpr int K2_G = 6 * K2_SLOT;
constexpr int K2_LDS = K2_G + 1024;
constexpr int AT_K = 3 * K2_SLOT;
constexpr int AT_V = AT_K + 32768;
constexpr int AT_V1 = AT_V + 16384 + 128;
static_assert(AT_V1 + 16384 <= K2_LDS, "lds");
#define NO_ 0
constexpr int NH = 28, NO = NO_, NK = NH + NO, NC = 16 - 2 * NO, NS = NK + NC;

__device__ __forceinline__ void st16_sc1(void* p, u32x4 w) {
  asm volatile("global_store_dwordx4 %0, %1, off sc1\n\ts_nop 1" ::"v"(p), "v"(w) : "memory");
}
template <int SC1>
__device__ __forceinline__ void glds16x4(const void* sbase, unsigned voff, unsigned lds_addr) {
  if (SC1)
    asm volatile("s_mov_b32 m0, %2\n\ts_nop 0\n\tglobal_load_lds_dwordx4 %0, %1 sc1\n\tglobal_load_lds_dwordx4 %0, %1 offset:1024 sc1\n\t"
                 "global_load_lds_dwordx4 %0, %1 offset:2048 sc1\n\tglobal_load_lds_dwordx4 %0, %1 offset:3072 sc1" ::"v"(voff), "s"(sbase), "s"(lds_addr) : "memory");
  else
    asm volatile("s_mov_b32 m0, %2\n\ts_nop 0\n\tglobal_load_lds_dwordx4 %0, %1\n\tglobal_load_lds_dwordx4 %0, %1 offset:1024\n\t"
                 "global_load_lds_dwordx4 %0, %1 offset:2048\n\tglobal_load_lds_dwordx4 %0, %1 offset:3072" ::"v"(voff), "s"(sbase), "s"(lds_addr) : "memory");
}
__device__ __forceinline__ void glds16x2(const void* sbase, unsigned voff0, unsigned voff1m, unsigned lds_addr) {
  asm volatile("s_mov_b32 m0, %3\n\ts_nop 0\n\tglobal_load_lds_dwordx4 %0, %2\n\tglobal_load_lds_dwordx4 %1, %2 offset:1024" ::"v"(voff0), "v"(voff1m),
               "s"(sbase), "s"(lds_addr) : "memory");
}
#define LP(a) ((const char LAS*)(uintptr_t)(a))

__device__ __forceinline__ float vsub(float a, float b) { float r; asm("v_sub_f32 %0, %1, %2" : "=v"(r) : "v"(a), "v"(b)); return r; }
__device__ __forceinline__ float vmul(float a, float b) { float r; asm("v_mul_f32 %0, %1, %2" : "=v"(r) : "v"(a), "v"(b)); return r; }
template <bool FIRST, class KW, class VL, class VW, class PF>
__device__ __forceinline__ void attn_wave(const char LAS* lds, char* ws, int wid, int lane, int head, int qrow, const bf16x8 (&qf)[2], const float& gval,
                                          KW kwait, VL vload, VW vwait, PF pf) {
  const int r32 = lane & 31, hi = lane >> 5;
  const int kb0 = FIRST ? 8 - wid : 0;
  const int dq = r32 - 4 * hi;
  const char LAS* kbase = lds + AT_K + wid * 1024 + r32 * 32 + ((hi ^ ((r32 >> 3) & 1)) << 4);
  const f32x16 zero = {0.f, 0.f, 0.f, 0.f, 0.f, 0.f, 0.f, 0.f, 0.f, 0.f, 0.f, 0.f, 0.f, 0.f, 0.f, 0.f};
  bf16x8 kf[9][2];
  f32x16 sb[3];
#define QKA(kb, dst, basev) dst = __builtin_amdgcn_mfma_f32_32x32x16_bf16(kf[kb][0], qf[0], (kb) == 0 ? mk0 : ((kb) == 8 ? mk8 : basev), 0, 0, 0)
#define QKB(kb, dst) dst = __builtin_amdgcn_mfma_f32_32x32x16_bf16(kf[kb][1], qf[1], dst, 0, 0, 0)
#define MX3(a, b, c) __builtin_fmaxf(__builtin_fmaxf(a, b), c)
  f32x16 mk0, mk8;
#pragma unroll
  for (int r = 0; r < 16; ++r) { const bool ab = ((r & 3) + 8 * (r >> 2)) > dq; mk0[r] = ab ? 0.0f : -INFINITY; mk8[r] = ab ? -INFINITY : 0.0f; }
  bf16x8 sel;
  { const short one = (((lane >> 4) & 1) == (lane & 1)) ? (short)0x3F80 : (short)0;
    sel = (bf16x8){one, one, one, one, one, one, one, one}; }
  const char LAS* vbase;
  { const int g = lane >> 4, q = (lane & 15) >> 2, p = lane & 3;
    vbase = lds + ((g & 1) ? AT_V1 : AT_V) + wid * 1024 + (4 * (g >> 1) + q) * 32 + p * 8; }
  u16* optr = (u16*)(ws + WS_A2) + ((size_t)(224 + head * 2) * S_ + qrow) * 16 + hi * 8;
  asm volatile("" : "+v"(mk0), "+v"(mk8), "+v"(sel), "+v"(vbase), "+v"(kbase), "+v"(optr));
  kwait();
#pragma unroll
  for (int kb = 0; kb < 9; ++kb) { kf[kb][0] = lds_b128(kbase + kb * 1024); kf[kb][1] = lds_b128(kbase + 16384 + kb * 1024); }
  vload();
  float ma = -INFINITY, mb = -INFINITY;
  SCHED;
#pragma unroll
  for (int kb = 0; kb < 11; ++kb) {
    const int pb = kb - 2;
    float a_ = ma, b_ = mb;
    if (kb < 9) { QKA(kb, sb[kb % 3], zero); SCHED; }
    if (pb >= 0) { const f32x16& t_ = sb[pb % 3]; a_ = MX3(a_, t_[0], t_[1]); b_ = MX3(b_, t_[2], t_[3]); a_ = MX3(a_, t_[4], t_[5]); b_ = MX3(b_, t_[6], t_[7]); SCHED; }
    if (kb < 9) { QKB(kb, sb[kb % 3]); SCHED; }
    if (pb >= 0) { const f32x16& t_ = sb[pb % 3]; a_ = MX3(a_, t_[8], t_[9]); b_ = MX3(b_, t_[10], t_[11]); a_ = MX3(a_, t_[12], t_[13]); b_ = MX3(b_, t_[14], t_[15]);
      if (FIRST) { ma = pb >= kb0 ? a_ : ma; mb = pb >= kb0 ? b_ : mb; } else { ma = a_; mb = b_; } }
    SCHED;
  }
  float m = __builtin_fmaxf(ma, mb);
  { auto rr = __builtin_amdgcn_permlane32_swap(__float_as_uint(m), __float_as_uint(m), false, false);
    m = __builtin_fmaxf(__uint_as_float(rr[0]), __uint_as_float(rr[1])); }
  f32x16 cm;
#pragma unroll
  for (int r = 0; r < 16; ++r) mk0[r] = vsub(mk0[r], m);
  asm volatile("s_nop 1" : "+v"(mk0));
  QKA(0, sb[0], cm); SCHED;
#pragma unroll
  for (int r = 0; r < 16; ++r) cm[r] = -m;
  asm volatile("" : "+v"(cm));
  QKB(0, sb[0]); SCHED;
#pragma unroll
  for (int r = 0; r < 16; ++r) mk8[r] = vsub(mk8[r], m);
  vwait();
  f32x16 o = zero;
  f32x4 lsum = {0.f, 0.f, 0.f, 0.f};
  s16x4 vf[2][4]; unsigned pk[8]; float ex[2][2];
#define SB2(kb) sb[(kb) & 1]
#define VF(kb) do { _Pragma("unroll") for (int i_ = 0; i_ < 4; ++i_) vf[(kb) & 1][i_] = lds_tr(vbase + (kb) * 1024 + i_ * 256); } while (0)
#define PW(st) __builtin_bit_cast(bf16x8, (u32x4){pk[4 * (st)], pk[4 * (st) + 1], pk[4 * (st) + 2], pk[4 * (st) + 3]})
#define PVM(kb, st) do { if (!FIRST || (kb) >= kb0) o = __builtin_amdgcn_mfma_f32_32x32x16_bf16(cat8(vf[(kb) & 1][2 * (st)], vf[(kb) & 1][2 * (st) + 1]), PW(st), o, 0, 0, 0); } while (0)
#define SUM(kb, st) do { if (!FIRST || (kb) >= kb0) lsum = __builtin_amdgcn_mfma_f32_16x16x32_bf16(sel, PW(st), lsum, 0, 0, 0); } while (0)
  VF(0);
#pragma unroll
  for (int kb = 0; kb < 9; ++kb) {
#pragma unroll
    for (int j = 0; j < 8; ++j) {
      ex[j & 1][0] = __builtin_amdgcn_exp2f(SB2(kb)[2 * j]); ex[j & 1][1] = __builtin_amdgcn_exp2f(SB2(kb)[2 * j + 1]);
      asm volatile("" : "+v"(ex[j & 1][0]), "+v"(ex[j & 1][1])); SCHED;
      if (kb + j > 0) { pk[(j + 7) & 7] = cvtpk(ex[(j + 1) & 1][0], ex[(j + 1) & 1][1]); asm volatile("" : "+v"(pk[(j + 7) & 7])); }
      if (j == 0 && kb + 1 < 9) QKA(kb + 1, SB2(kb + 1), cm);
      if (j == 1 && kb > 0) PVM(kb - 1, 1);
      if (j == 2 && kb > 0) SUM(kb - 1, 1);
      if (j == 3 && kb + 1 < 9) QKB(kb + 1, SB2(kb + 1));
      if (j == 3 && kb + 1 < 9) VF(kb + 1);
      if (j == 5) PVM(kb, 0);
      if (j == 6) pf(kb);
      if (j == 7) SUM(kb, 0);
      SCHED;
    }
  }
  pk[7] = cvtpk(ex[1][0], ex[1][1]);
  PVM(8, 1); SUM(8, 1);
  const float l = (lane & 16) ? lsum[1] : lsum[0];
  float sc = gval * __builtin_amdgcn_rcpf(l);
  { float o0_ = o[0] + 0.0f; asm volatile("s_nop 7\n\ts_nop 7" : "+v"(sc), "+v"(o0_)); }
#pragma unroll
  for (int dh = 0; dh < 2; ++dh) {
    const unsigned a0 = cvtpk(vmul(o[8 * dh + 0], sc), vmul(o[8 * dh + 1], sc)), a1 = cvtpk(vmul(o[8 * dh + 2], sc), vmul(o[8 * dh + 3], sc));
    const unsigned b0 = cvtpk(vmul(o[8 * dh + 4], sc), vmul(o[8 * dh + 5], sc)), b1 = cvtpk(vmul(o[8 * dh + 6], sc), vmul(o[8 * dh + 7], sc));
    auto r0 = __builtin_amdgcn_permlane32_swap(a0, b0, false, false);
    auto r1 = __builtin_amdgcn_permlane32_swap(a1, b1, false, false);
    u32x4 w = {r0[0], r1[0], r0[1], r1[1]};
    st16_sc1(optr + (size_t)dh * S_ * 16, w);
  }
#undef QKA
#undef QKB
#undef VF
#undef PW
#undef PVM
#undef SUM
#undef SB2
}

__device__ __forceinline__ void wait_count(unsigned* c, unsigned want) {
  unsigned spins = 0;
  while ((unsigned)__builtin_amdgcn_readfirstlane(__hip_atomic_load(c, __ATOMIC_RELAXED, __HIP_MEMORY_SCOPE_AGENT)) < want) {
    __builtin_amdgcn_s_sleep(1);
    if (++spins > (1u << 22)) break;
  }
  asm volatile("" ::: "memory");
}

__global__ __launch_bounds__(512, 2) void k2_fused(const u16* __restrict__ wd, const u16* __restrict__ wo, char* __restrict__ ws,
                                                   u16* __restrict__ out) {
  extern __shared__ __attribute__((aligned(16))) char lds_[];
  char LAS* lds = (char LAS*)lds_;
  const unsigned lds0 = (unsigned)(uintptr_t)lds;
  const int tid = threadIdx.x, lane = tid & 63;
  const int wid = __builtin_amdgcn_readfirstlane(tid >> 6);
  const int bid = blockIdx.x;
  const int xcd = bid & 7, idx = bid >> 3;
  const int sp = xcd >> 1, tn = idx & 7, tm = (xcd & 1) * 4 + (idx >> 3);
  const int mp = sp >> 1, np = sp & 1;
  const int tile = tm * 8 + tn;
  const int head = sp * 8 + tn, rb = tm;
  const int q0 = rb * 256;
  const char* qkv = ws + WS_QKV;
  unsigned* cntA = (unsigned*)(ws + WS_CNT);
  unsigned* cntP = (unsigned*)(ws + WS_CNT + 16 * 128);
  STAMP_DECL;
  STAMP(1, 0);

  const int qrow = q0 + wid * 32 + (lane & 31);
  const int rowlo = (rb > 0 ? q0 - 256 : 0) + wid * 32, rowhi = q0 + wid * 32;
  bf16x8 qf[2];
  float gval;
  {
    const char* qp = qkv + ((size_t)(head * 2) * S_ + q0 + wid * 32) * 32;
    const unsigned voffQ = (lane & 31) * 32 + (lane >> 5) * 16;
    qf[0] = *(const bf16x8*)(qp + voffQ);
    qf[1] = *(const bf16x8*)(qp + S_ * 32 + voffQ);
    gval = ((const float*)(ws + WS_G))[qrow];
    const unsigned voffK = (lane >> 1) * 32 + (((lane & 1) ^ ((lane >> 4) & 1)) << 4);

#pragma unroll
    for (int dh = 0; dh < 2; ++dh) {
      glds16(qkv + ((size_t)(64 + head * 2 + dh) * S_ + rowhi) * 32, voffK, lds0 + AT_K + dh * 16384 + (wid + 8) * 1024);
      glds16(qkv + ((size_t)(64 + head * 2 + dh) * S_ + rowlo) * 32, voffK, lds0 + AT_K + dh * 16384 + wid * 1024);
    }
  }

  const int ld = wid & 3;
  unsigned voffA, voffB0, voffB1m;
  auto set_voff = [&]() {
    const int l_ = fresh_lane();
    const int r = l_ >> 3, j = l_ & 7, q = r & 3;
    const int f0 = (q >> 1) & 1, f1 = f0 | 2;
    voffA = l_ * 16;
    voffB0 = r * 2048 + ((((j >> 1) ^ f0) << 1 | (j & 1)) << 4);
    voffB1m = (r + 8) * 2048 + ((((j >> 1) ^ f1) << 1 | (j & 1)) << 4) - 1024;
  };
  const char* hA = ws + WS_A2 + ((size_t)(sp * NH * 2 + (ld >> 1)) * S_ + tm * 256 + ((ld & 1) ^ mp) * 128) * 32;
  const char* hB = (const char*)wd + (size_t)(sp * NH * 32 + 16 * (ld & 1)) * 2048 + (size_t)(tn * 128 + ((ld >> 1) ^ np) * 64) * 2;
  const char* oA = ws + WS_A2 + ((size_t)(224 + sp * NO * 2 + (ld >> 1)) * S_ + tm * 256 + ((ld & 1) ^ mp) * 128) * 32;
  const char* oB = (const char*)wo + (size_t)(sp * NO * 32 + 16 * (ld & 1)) * 2048 + (size_t)(tn * 128 + ((ld >> 1) ^ np) * 64) * 2;
  const char* cA = ws + WS_A2 + ((size_t)(224 + 8 * NO + ld) * S_ + tm * 256 + mp * 128) * 32;
  const char* cB = (const char*)wo + (size_t)(128 * NO + 16 * ld) * 2048 + (size_t)(tn * 128 + np * 64) * 2;
  auto issue = [&](int i) {
    const unsigned dst = lds0 + (i % 6) * K2_SLOT;
    if (i < NH) {
      glds16x4<0>(hA + (size_t)i * (2 * S_ * 32), voffA, dst + ld * 4096);
      glds16x2(hB + (size_t)i * (32 * 2048), voffB0, voffB1m, dst + 16384 + ld * 2048);
    } else if (i < NK) {
      glds16x4<1>(oA + (size_t)(i - NH) * (2 * S_ * 32), voffA, dst + ld * 4096);
      glds16x2(oB + (size_t)(i - NH) * (32 * 2048), voffB0, voffB1m, dst + 16384 + ld * 2048);
    } else {
      glds16x4<1>(cA + (size_t)(i - NK) * (4 * S_ * 32), voffA, dst + ld * 4096);
      glds16x2(cB + (size_t)(i - NK) * (64 * 2048), voffB0, voffB1m, dst + 16384 + ld * 2048);
    }
  };

  {
    set_voff();
    const int hf = wid >> 2;
    unsigned voffBp = hf ? voffB1m + 1024 : voffB0;
    const char* pA = hA + hf * 2048;
    const unsigned dA = lds0 + ld * 4096 + hf * 2048, dB = lds0 + 16384 + ld * 2048 + hf * 1024;
    auto kwait = [&]() {
      asm volatile("" : "+v"(voffA), "+v"(voffBp));
      asm volatile("s_waitcnt vmcnt(0)" : "+v"(qf[0]), "+v"(qf[1]), "+v"(gval) :: "memory");
      BAR;
      asm volatile("" ::: "memory");
      STAMP(1, 1);
    };
    auto vload = [&]() {
#pragma unroll
      for (int dh = 0; dh < 2; ++dh) {
        glds16(qkv + ((size_t)(128 + head * 2 + dh) * S_ + rowlo) * 32, voffA, lds0 + (dh ? AT_V1 : AT_V) + wid * 1024);
        glds16(qkv + ((size_t)(128 + head * 2 + dh) * S_ + rowhi) * 32, voffA, lds0 + (dh ? AT_V1 : AT_V) + (wid + 8) * 1024);
      }
      if (lane < 32) *(LAS float*)(lds + K2_G + (wid * 32 + lane) * 4) = 1.0f - gval;
    };
    auto vwait = [&]() { WAIT_V(0); BAR; asm volatile("" ::: "memory"); };
    auto pf = [&](int kb) {
      const int st = kb / 3, j = kb % 3;
      if (j < 2) glds16(pA + (size_t)st * (2 * S_ * 32) + j * 1024, voffA, dA + st * K2_SLOT + j * 1024);
      else glds16(hB + (size_t)st * (32 * 2048), voffBp, dB + st * K2_SLOT);
    };
    if (rb == 0) attn_wave<true>(lds, ws, wid, lane, head, qrow, qf, gval, kwait, vload, vwait, pf);
    else attn_wave<false>(lds, ws, wid, lane, head, qrow, qf, gval, kwait, vload, vwait, pf);
  }
  STAMP(1, 2);

  if (wid >= 4) {
    set_voff();
    WAIT_V(8);
    WAIT_L0; BAR;
    issue(3); issue(4);
    WAIT_V(14);
    BAR;
    unsigned* ca = cntA + tm * 32;
    unsigned fl = 0;
    for (int P = 0; P < NS / 2; ++P) {
      const int a = 2 * P + 5;
      if (P == 9) asm volatile("global_load_dword %0, %1, off sc1" : "=v"(fl) : "v"(ca) : "memory");
      if (P == 11) {
        asm volatile("" : "+v"(fl));
        if ((unsigned)__builtin_amdgcn_readfirstlane(fl) < 256u) wait_count(ca, 256u);
      }
      if (a + 1 < NS) { issue(a); issue(a + 1); WAIT_V(12); }
      else if (a < NS) { issue(a); WAIT_V(6); }
      else { WAIT_V(0); }
      if (P == 0 && voffA == 0) __hip_atomic_fetch_add(ca, 1u, __ATOMIC_RELAXED, __HIP_MEMORY_SCOPE_AGENT);
      if (P + 1 < NS / 2) BAR;
    }
    STAMP(1, 3); STAMP(1, 4); STAMP(1, 5); STAMP(1, 6);
    STAMP_END(1, 7);
    return;
  }

  const int wm = wid & 1, wn = wid >> 1;
  const int fr = lane & 15, g4 = lane >> 4;
  const unsigned rAh = lds0 + (g4 >> 1) * 8192 + (wm * 64 + fr) * 32 + (g4 & 1) * 16;
  unsigned rAc = 0;
  unsigned rB0, rB1;
  { const int q = (lane & 15) >> 2, p = lane & 3;
    const int f = ((q >> 1) & 1) | ((g4 & 1) << 1);
    rB0 = lds0 + 16384 + g4 * 1024 + q * 128 + (((wn * 2) ^ f) << 5) + p * 8;
    rB1 = rB0 ^ 32u; }

  f32x4 acc[8][4];
#pragma unroll
  for (int m = 0; m < 8; ++m)
#pragma unroll
    for (int n = 0; n < 4; ++n) acc[m][n] = (f32x4){0.f, 0.f, 0.f, 0.f};
  bf16x8 A0[8], A1[8];
  s16x4 L0[4], H0[4], L1[4], H1[4];
#define RD(AF, BL, BH, k, so, COV) do { \
    if ((k) < 8) { const int n_ = (k) >> 1; const unsigned b_ = ((n_ & 1) ? rB1 : rB0) + (so); \
      if ((k) & 1) BH[n_] = lds_tr(LP(b_) + (n_ >> 1) * 4096 + 512); else BL[n_] = lds_tr(LP(b_) + (n_ >> 1) * 4096); } \
    else { const int m_ = (k) - 8; \
      if (COV) AF[m_] = lds_b128(LP(rAc + (so)) + (m_ >> 2) * 8192 + (m_ & 3) * 512); \
      else AF[m_] = lds_b128(LP(rAh + (so)) + (m_ >> 2) * 4096 + (m_ & 3) * 512); } } while (0)
#define MFH(AF, BL, BH, k) acc[(k) >> 2][(k) & 3] = __builtin_amdgcn_mfma_f32_16x16x32_bf16(cat8(BL[(k) & 3], BH[(k) & 3]), AF[(k) >> 2], acc[(k) >> 2][(k) & 3], 0, 0, 0)
#define STAGE_H(AC, LC, HC, AN, LN, HN, so, COV) do { \
    _Pragma("unroll") for (int k = 0; k < 32; ++k) { MFH(AC, LC, HC, k); if (k >= 1 && k < 17) RD(AN, LN, HN, k - 1, so, COV); SCHED; } } while (0)

  WAIT_V(8); WAIT_L0; BAR;
  asm volatile("" ::: "memory");
#pragma unroll
  for (int k = 0; k < 16; ++k) RD(A0, L0, H0, k, 0, 0);
  WAIT_V(2); WAIT_L0; BAR;
  SCHED;
  STAMP(1, 3);
  int s1 = 1;
#pragma unroll 1
  for (int ph = 0; ph < 2; ++ph) {
    const int np_ = ph ? (NO > 0 ? NO / 2 - 1 : 0) : (NO > 0 ? NH / 2 : NH / 2 - 1);
#pragma unroll 1
    for (int P = 0; P < np_; ++P) {
      const int s2 = (s1 == 5) ? 0 : s1 + 1;
      STAGE_H(A0, L0, H0, A1, L1, H1, s1 * K2_SLOT, 0);
      STAGE_H(A1, L1, H1, A0, L0, H0, s2 * K2_SLOT, 0);
      s1 = s2 + 1;
      if (P == 2) { WAIT_V(0); if (fresh_lane() == 0) __hip_atomic_fetch_add(cntA + tm * 32, 1u, __ATOMIC_RELAXED, __HIP_MEMORY_SCOPE_AGENT); }
      WAIT_L0; BAR; SCHED;
    }
    if (ph == 0 && NO > 0) {
      const int l4 = fresh_lane();
#pragma unroll
      for (int m = 0; m < 8; ++m) {
        const float sc = *(const LAS float*)(lds + K2_G + (((m >> 2) ^ mp) * 128 + wm * 64 + (m & 3) * 16 + (l4 & 15)) * 4);
#pragma unroll
        for (int n = 0; n < 4; ++n) acc[m][n] *= sc;
      }
      SCHED;
    }
  }
  STAGE_H(A0, L0, H0, A1, L1, H1, ((NK - 1) % 6) * K2_SLOT, 0);
  { const int l2 = fresh_lane(); rAc = lds0 + (l2 >> 5) * 4096 + (wm * 64 + (l2 & 15)) * 32 + ((l2 >> 4) & 1) * 16; }
  STAGE_H(A1, L1, H1, A0, L0, H0, (NK % 6) * K2_SLOT, 1);
  WAIT_L0; BAR; SCHED;
  STAMP(1, 4);

  auto rs = __builtin_amdgcn_make_buffer_rsrc((void*)(ws + WS_PART), 0, 16u << 20, 0x00020000);
  const int l3 = fresh_lane();
  if (NO == 0) {
#pragma unroll
    for (int m = 0; m < 8; ++m) {
      const float sc = *(const LAS float*)(lds + K2_G + (((m >> 2) ^ mp) * 128 + wm * 64 + (m & 3) * 16 + (l3 & 15)) * 4);
#pragma unroll
      for (int n = 0; n < 4; ++n) acc[m][n] *= sc;
    }
  }
#pragma unroll
  for (int d = 3; d >= 1; --d) {
    const int h = d >> 1, c = d & 1;
#pragma unroll
    for (int mb = 0; mb < 4; ++mb) {
      const f32x4 u = acc[4 * h + mb][2 * c], v = acc[4 * h + mb][2 * c + 1];
      u32x4 w = {cvtpk(u[0], u[1]), cvtpk(u[2], u[3]), cvtpk(v[0], v[1]), cvtpk(v[2], v[3])};
      const unsigned off = (((((unsigned)tile * 4 + (d ^ sp)) * 4 + sp) * 4 + wid) * 4 + mb) * 1024u + l3 * 16;
      __builtin_amdgcn_raw_buffer_store_b128(w, rs, off, 0, 16);
    }
  }
  f32x4 acK[4][2];
#pragma unroll
  for (int mb = 0; mb < 4; ++mb)
#pragma unroll
    for (int j = 0; j < 2; ++j) acK[mb][j] = acc[mb][j];
  SCHED;
#define MFC(AF, BL, BH, k) acK[((k) >> 1) & 3][(k) & 1] = __builtin_amdgcn_mfma_f32_16x16x32_bf16(cat8(BL[2 * ((k) >> 3) + ((k) & 1)], BH[2 * ((k) >> 3) + ((k) & 1)]), \
                                                             AF[4 * ((k) >> 3) + (((k) >> 1) & 3)], acK[((k) >> 1) & 3][(k) & 1], 0, 0, 0)
#define STAGE_C(AC, LC, HC, AN, LN, HN, so) do { \
    _Pragma("unroll") for (int k = 0; k < 16; ++k) { MFC(AC, LC, HC, k); RD(AN, LN, HN, k, so, 1); SCHED; } } while (0)
  u32x4 part[3][4];
  unsigned* myc = cntP + (tile * 4 + wid) * 32;
  unsigned flagv = 0;
#define XSIG 2
#define XLD 2
#pragma unroll
  for (int P = NK / 2; P < NS / 2; ++P) {
    STAGE_C(A0, L0, H0, A1, L1, H1, ((2 * P + 1) % 6) * K2_SLOT);
    if (P + 1 < NS / 2) { STAGE_C(A1, L1, H1, A0, L0, H0, ((2 * P + 2) % 6) * K2_SLOT); }
    else {
#pragma unroll
      for (int k = 0; k < 16; ++k) MFC(A1, L1, H1, k);
    }
    if (P == NK / 2 + XSIG) { WAIT_V(0); if (l3 == 0) __hip_atomic_fetch_add(myc, 1u, __ATOMIC_RELAXED, __HIP_MEMORY_SCOPE_AGENT); }
    if (P == NS / 2 - 2 - XLD) flagv = __hip_atomic_load(myc, __ATOMIC_RELAXED, __HIP_MEMORY_SCOPE_AGENT);
    if (P + 1 < NS / 2) { WAIT_L0; BAR; SCHED; }
    if (P == NS / 2 - 1 - XLD) {
      if ((unsigned)__builtin_amdgcn_readfirstlane(flagv) < 4u) wait_count(myc, 4u);
      asm volatile("" ::: "memory");
#pragma unroll
      for (int jj = 0; jj < 3; ++jj) {
        const int src = (sp + 1 + jj) & 3;
#pragma unroll
        for (int mb = 0; mb < 4; ++mb) {
          const unsigned off = (((((unsigned)tile * 4 + sp) * 4 + src) * 4 + wid) * 4 + mb) * 1024u + l3 * 16;
          part[jj][mb] = __builtin_amdgcn_raw_buffer_load_b128(rs, off, 0, 16);
        }
      }
    }
  }
  STAMP(1, 5);
  const int lane_e = fresh_lane();
#pragma unroll
  for (int mb = 0; mb < 4; ++mb) {
    f32x4 u = acK[mb][0], v = acK[mb][1];
#pragma unroll
    for (int jj = 0; jj < 3; ++jj) {
      const u32x4 w = part[jj][mb];
      u[0] += __uint_as_float(w[0] << 16); u[1] += __uint_as_float(w[0] & 0xffff0000u);
      u[2] += __uint_as_float(w[1] << 16); u[3] += __uint_as_float(w[1] & 0xffff0000u);
      v[0] += __uint_as_float(w[2] << 16); v[1] += __uint_as_float(w[2] & 0xffff0000u);
      v[2] += __uint_as_float(w[3] << 16); v[3] += __uint_as_float(w[3] & 0xffff0000u);
    }
    auto r0 = __builtin_amdgcn_permlane16_swap(cvtpk(u[0], u[1]), cvtpk(v[0], v[1]), false, false);
    auto r1 = __builtin_amdgcn_permlane16_swap(cvtpk(u[2], u[3]), cvtpk(v[2], v[3]), false, false);
    u32x4 w = {r0[0], r1[0], r0[1], r1[1]};
    const int row = tm * 256 + mp * 128 + wm * 64 + mb * 16 + (lane_e & 15);
    const int col = tn * 128 + np * 64 + wn * 32 + ((lane_e >> 4) & 1) * 16 + (lane_e >> 5) * 8;
    *(u32x4*)(out + (size_t)row * D_ + col) = w;
  }
  STAMP(1, 6);
  STAMP_END(1, 7);
}

extern "C" void kernel_launch(void* const* d_in, const int* in_sizes, int n_in, void* d_out, int out_size, void* d_ws, size_t ws_size,
                              hipStream_t stream) {
  const u16* x = (const u16*)d_in[0];
  const u16* wqkv = (const u16*)d_in[1];
  const u16* wo = (const u16*)d_in[2];
  const u16* wg = (const u16*)d_in[4];
  const u16* wu = (const u16*)d_in[5];
  const u16* wd = (const u16*)d_in[6];
  const u16* gate_w = (const u16*)d_in[7];
  char* ws = (char*)d_ws;
  (void)hipFuncSetAttribute((const void*)k_gemm1, hipFuncAttributeMaxDynamicSharedMemorySize, G1_LDS);
  (void)hipFuncSetAttribute((const void*)k2_fused, hipFuncAttributeMaxDynamicSharedMemorySize, K2_LDS);
  k_gemm1<<<256, 512, G1_LDS, stream>>>(x, wqkv, wg, wu, gate_w, ws, wd, wo, (const u16*)d_out);
  k2_fused<<<256, 512, K2_LDS, stream>>>(wd, wo, ws, (u16*)d_out);
}
```

```cpp
#include <hip/hip_runtime.h>
#include <hip/hip_bf16.h>
#include <stdint.h>

#define LAS __attribute__((address_space(3)))
typedef unsigned short u16;
typedef short bf16x8 __attribute__((ext_vector_type(8)));
typedef short s16x4 __attribute__((ext_vector_type(4)));
typedef float f32x4 __attribute__((ext_vector_type(4)));
typedef float f32x16 __attribute__((ext_vector_type(16)));
typedef unsigned u32x4 __attribute__((ext_vector_type(4)));
typedef unsigned u32x2 __attribute__((ext_vector_type(2)));

constexpr int S_ = 2048, D_ = 1024, F_ = 3584, NQKV = 3072, K2 = F_ + D_;
constexpr float QSCALE = 0.17677669529663687f * 1.4426950408889634f;

constexpr size_t WS_QKV = 0;
constexpr size_t WS_A2 = WS_QKV + (size_t)NQKV * S_ * 2;
constexpr size_t WS_G = WS_A2 + (size_t)K2 * S_ * 2;
constexpr size_t WS_CNT = WS_G + S_ * 4;
constexpr size_t WS_PART = WS_CNT + 65536;

#define WAIT_V(n) asm volatile("s_waitcnt vmcnt(" #n ")" ::: "memory")
#define WAIT_L0 asm volatile("s_waitcnt lgkmcnt(0)" ::: "memory")
#define BAR __builtin_amdgcn_s_barrier()
#define SCHED __builtin_amdgcn_sched_barrier(0)

#define STAMP_DECL do {} while (0)
#define STAMP(kid, i) do {} while (0)
#define STAMP_END(kid, n) do {} while (0)
__device__ __forceinline__ int fresh_lane() {
  int l;
  asm volatile("v_mbcnt_lo_u32_b32 %0, -1, 0\n\tv_mbcnt_hi_u32_b32 %0, -1, %0" : "=v"(l));
  return l;
}
__device__ __forceinline__ void glds16(const void* sbase, unsigned voff, unsigned lds_addr) {
  asm volatile("s_mov_b32 m0, %2\n\ts_nop 0\n\tglobal_load_lds_dwordx4 %0, %1" ::"v"(voff), "s"(sbase), "s"(lds_addr) : "memory");
}
typedef __bf16 bf16x2_t __attribute__((ext_vector_type(2)));
typedef float f32x2 __attribute__((ext_vector_type(2)));
__device__ __forceinline__ unsigned cvtpk(float lo, float hi) {
  return __builtin_bit_cast(unsigned, __builtin_convertvector((f32x2){lo, hi}, bf16x2_t));
}
__device__ __forceinline__ bf16x8 lds_b128(const char LAS* p) { return *(const LAS bf16x8*)p; }
__device__ __forceinline__ s16x4 lds_tr(const char LAS* p) {
  return __builtin_bit_cast(s16x4, __builtin_amdgcn_ds_read_tr16_b64_v4i16((LAS s16x4*)p));
}
__device__ __forceinline__ bf16x8 cat8(s16x4 a, s16x4 b) {
  return (bf16x8){a[0], a[1], a[2], a[3], b[0], b[1], b[2], b[3]};
}
__device__ __forceinline__ f32x2 geglu2(f32x2 x, f32x2 u) {
  const float c0 = -2.0f * 0.7978845608028654f * 1.4426950408889634f;
  const float c1 = c0 * 0.044715f;
  const f32x2 t = x * x;
  const f32x2 z = x * (t * c1 + c0);
  f32x2 e; e.x = __builtin_amdgcn_exp2f(z.x); e.y = __builtin_amdgcn_exp2f(z.y);
  const f32x2 d = e + 1.0f;
  f32x2 r; r.x = __builtin_amdgcn_rcpf(d.x); r.y = __builtin_amdgcn_rcpf(d.y);
  return (x * u) * r;
}
__device__ __forceinline__ void glds4(const void* sbase, unsigned voff, unsigned lds_addr) {
  asm volatile("s_mov_b32 m0, %2\n\ts_nop 0\n\tglobal_load_lds_dword %0, %1" ::"v"(voff), "s"(sbase), "s"(lds_addr) : "memory");
}

constexpr int K2_SLOT = 24576;
constexpr int K2_G = 6 * K2_SLOT;
constexpr int K2_LDS = K2_G + 1024;
constexpr int AT_K = 3 * K2_SLOT;
constexpr int AT_V = AT_K + 32768;
constexpr int AT_V1 = AT_V + 16384 + 128;
static_assert(AT_V1 + 16384 <= K2_LDS, "lds");
#define NO_ 0
constexpr int NH = 28, NO = NO_, NK = NH + NO, NC = 16 - 2 * NO, NS = NK + NC;

__device__ __forceinline__ void st16_sc1(void* p, u32x4 w) {
  asm volatile("global_store_dwordx4 %0, %1, off sc1\n\ts_nop 1" ::"v"(p), "v"(w) : "memory");
}
template <int SC1>
__device__ __forceinline__ void glds16x4(const void* sbase, unsigned voff, unsigned lds_addr) {
  if (SC1)
    asm volatile("s_mov_b32 m0, %2\n\ts_nop 0\n\tglobal_load_lds_dwordx4 %0, %1 sc1\n\tglobal_load_lds_dwordx4 %0, %1 offset:1024 sc1\n\t"
                 "global_load_lds_dwordx4 %0, %1 offset:2048 sc1\n\tglobal_load_lds_dwordx4 %0, %1 offset:3072 sc1" ::"v"(voff), "s"(sbase), "s"(lds_addr) : "memory");
  else
    asm volatile("s_mov_b32 m0, %2\n\ts_nop 0\n\tglobal_load_lds_dwordx4 %0, %1\n\tglobal_load_lds_dwordx4 %0, %1 offset:1024\n\t"
                 "global_load_lds_dwordx4 %0, %1 offset:2048\n\tglobal_load_lds_dwordx4 %0, %1 offset:3072" ::"v"(voff), "s"(sbase), "s"(lds_addr) : "memory");
}
__device__ __forceinline__ void glds16x2(const void* sbase, unsigned voff0, unsigned voff1m, unsigned lds_addr) {
  asm volatile("s_mov_b32 m0, %3\n\ts_nop 0\n\tglobal_load_lds_dwordx4 %0, %2\n\tglobal_load_lds_dwordx4 %1, %2 offset:1024" ::"v"(voff0), "v"(voff1m),
               "s"(sbase), "s"(lds_addr) : "memory");
}
#define LP(a) ((const char LAS*)(uintptr_t)(a))

__device__ __forceinline__ float vsub(float a, float b) { float r; asm("v_sub_f32 %0, %1, %2" : "=v"(r) : "v"(a), "v"(b)); return r; }
__device__ __forceinline__ float vmul(float a, float b) { float r; asm("v_mul_f32 %0, %1, %2" : "=v"(r) : "v"(a), "v"(b)); return r; }
template <bool FIRST, class KW, class VL, class VW, class PF>
__device__ __forceinline__ void attn_wave(const char LAS* lds, char* ws, int wid, int lane, int head, int qrow, const bf16x8 (&qf)[2], const float& gval,
                                          KW kwait, VL vload, VW vwait, PF pf) {
  const int r32 = lane & 31, hi = lane >> 5;
  const int kb0 = FIRST ? 8 - wid : 0;
  const int dq = r32 - 4 * hi;
  const char LAS* kbase = lds + AT_K + wid * 1024 + r32 * 32 + ((hi ^ ((r32 >> 3) & 1)) << 4);
  const f32x16 zero = {0.f, 0.f, 0.f, 0.f, 0.f, 0.f, 0.f, 0.f, 0.f, 0.f, 0.f, 0.f, 0.f, 0.f, 0.f, 0.f};
  bf16x8 kf[9][2];
  f32x16 sb[3];
#define QKA(kb, dst, basev) dst = __builtin_amdgcn_mfma_f32_32x32x16_bf16(kf[kb][0], qf[0], (kb) == 0 ? mk0 : ((kb) == 8 ? mk8 : basev), 0, 0, 0)
#define QKB(kb, dst) dst = __builtin_amdgcn_mfma_f32_32x32x16_bf16(kf[kb][1], qf[1], dst, 0, 0, 0)
#define MX3(a, b, c) __builtin_fmaxf(__builtin_fmaxf(a, b), c)
  f32x16 mk0, mk8;
#pragma unroll
  for (int r = 0; r < 16; ++r) { const bool ab = ((r & 3) + 8 * (r >> 2)) > dq; mk0[r] = ab ? 0.0f : -INFINITY; mk8[r] = ab ? -INFINITY : 0.0f; }
  bf16x8 sel;
  { const short one = (((lane >> 4) & 1) == (lane & 1)) ? (short)0x3F80 : (short)0;
    sel = (bf16x8){one, one, one, one, one, one, one, one}; }
  const char LAS* vbase;
  { const int g = lane >> 4, q = (lane & 15) >> 2, p = lane & 3;
    vbase = lds + ((g & 1) ? AT_V1 : AT_V) + wid * 1024 + (4 * (g >> 1) + q) * 32 + p * 8; }
  u16* optr = (u16*)(ws + WS_A2) + ((size_t)(224 + head * 2) * S_ + qrow) * 16 + hi * 8;
  asm volatile("" : "+v"(mk0), "+v"(mk8), "+v"(sel), "+v"(vbase), "+v"(kbase), "+v"(optr));
  kwait();
#pragma unroll
  for (int kb = 0; kb < 9; ++kb) { kf[kb][0] = lds_b128(kbase + kb * 1024); kf[kb][1] = lds_b128(kbase + 16384 + kb * 1024); }
  vload();
  float ma = -INFINITY, mb = -INFINITY;
  SCHED;
#pragma unroll
  for (int kb = 0; kb < 11; ++kb) {
    const int pb = kb - 2;
    float a_ = ma, b_ = mb;
    if (kb < 9) { QKA(kb, sb[kb % 3], zero); SCHED; }
    if (pb >= 0) { const f32x16& t_ = sb[pb % 3]; a_ = MX3(a_, t_[0], t_[1]); b_ = MX3(b_, t_[2], t_[3]); a_ = MX3(a_, t_[4], t_[5]); b_ = MX3(b_, t_[6], t_[7]); SCHED; }
    if (kb < 9) { QKB(kb, sb[kb % 3]); SCHED; }
    if (pb >= 0) { const f32x16& t_ = sb[pb % 3]; a_ = MX3(a_, t_[8], t_[9]); b_ = MX3(b_, t_[10], t_[11]); a_ = MX3(a_, t_[12], t_[13]); b_ = MX3(b_, t_[14], t_[15]);
      if (FIRST) { ma = pb >= kb0 ? a_ : ma; mb = pb >= kb0 ? b_ : mb; } else { ma = a_; mb = b_; } }
    SCHED;
  }
  float m = __builtin_fmaxf(ma, mb);
  { auto rr = __builtin_amdgcn_permlane32_swap(__float_as_uint(m), __float_as_uint(m), false, false);
    m = __builtin_fmaxf(__uint_as_float(rr[0]), __uint_as_float(rr[1])); }
  f32x16 cm;
#pragma unroll
  for (int r = 0; r < 16; ++r) mk0[r] = vsub(mk0[r], m);
  asm volatile("s_nop 1" : "+v"(mk0));
  QKA(0, sb[0], cm); SCHED;
#pragma unroll
  for (int r = 0; r < 16; ++r) cm[r] = -m;
  asm volatile("" : "+v"(cm));
  QKB(0, sb[0]); SCHED;
#pragma unroll
  for (int r = 0; r < 16; ++r) mk8[r] = vsub(mk8[r], m);
  vwait();
  f32x16 o = zero;
  f32x4 lsum = {0.f, 0.f, 0.f, 0.f};
  s16x4 vf[2][4]; unsigned pk[8]; float ex[2][2];
#define SB2(kb) sb[(kb) & 1]
#define VF(kb) do { _Pragma("unroll") for (int i_ = 0; i_ < 4; ++i_) vf[(kb) & 1][i_] = lds_tr(vbase + (kb) * 1024 + i_ * 256); } while (0)
#define PW(st) __builtin_bit_cast(bf16x8, (u32x4){pk[4 * (st)], pk[4 * (st) + 1], pk[4 * (st) + 2], pk[4 * (st) + 3]})
#define PVM(kb, st) do { if (!FIRST || (kb) >= kb0) o = __builtin_amdgcn_mfma_f32_32x32x16_bf16(cat8(vf[(kb) & 1][2 * (st)], vf[(kb) & 1][2 * (st) + 1]), PW(st), o, 0, 0, 0); } while (0)
#define SUM(kb, st) do { if (!FIRST || (kb) >= kb0) lsum = __builtin_amdgcn_mfma_f32_16x16x32_bf16(sel, PW(st), lsum, 0, 0, 0); } while (0)
  VF(0);
#pragma unroll
  for (int kb = 0; kb < 9; ++kb) {
#pragma unroll
    for (int j = 0; j < 8; ++j) {
      ex[j & 1][0] = __builtin_amdgcn_exp2f(SB2(kb)[2 * j]); ex[j & 1][1] = __builtin_amdgcn_exp2f(SB2(kb)[2 * j + 1]);
      asm volatile("" : "+v"(ex[j & 1][0]), "+v"(ex[j & 1][1])); SCHED;
      if (kb + j > 0) { pk[(j + 7) & 7] = cvtpk(ex[(j + 1) & 1][0], ex[(j + 1) & 1][1]); asm volatile("" : "+v"(pk[(j + 7) & 7])); }
      if (j == 0 && kb + 1 < 9) QKA(kb + 1, SB2(kb + 1), cm);
      if (j == 1 && kb > 0) PVM(kb - 1, 1);
      if (j == 2 && kb > 0) SUM(kb - 1, 1);
      if (j == 3 && kb + 1 < 9) QKB(kb + 1, SB2(kb + 1));
      if (j == 3 && kb + 1 < 9) VF(kb + 1);
      if (j == 5) PVM(kb, 0);
      if (j == 6) pf(kb);
      if (j == 7) SUM(kb, 0);
      SCHED;
    }
  }
  pk[7] = cvtpk(ex[1][0], ex[1][1]);
  PVM(8, 1); SUM(8, 1);
  const float l = (lane & 16) ? lsum[1] : lsum[0];
  float sc = gval * __builtin_amdgcn_rcpf(l);
  { float o0_ = o[0] + 0.0f; asm volatile("s_nop 7\n\ts_nop 7" : "+v"(sc), "+v"(o0_)); }
#pragma unroll
  for (int dh = 0; dh < 2; ++dh) {
    const unsigned a0 = cvtpk(vmul(o[8 * dh + 0], sc), vmul(o[8 * dh + 1], sc)), a1 = cvtpk(vmul(o[8 * dh + 2], sc), vmul(o[8 * dh + 3], sc));
    const unsigned b0 = cvtpk(vmul(o[8 * dh + 4], sc), vmul(o[8 * dh + 5], sc)), b1 = cvtpk(vmul(o[8 * dh + 6], sc), vmul(o[8 * dh + 7], sc));
    auto r0 = __builtin_amdgcn_permlane32_swap(a0, b0, false, false);
    auto r1 = __builtin_amdgcn_permlane32_swap(a1, b1, false, false);
    u32x4 w = {r0[0], r1[0], r0[1], r1[1]};
    st16_sc1(optr + (size_t)dh * S_ * 16, w);
  }
#undef QKA
#undef QKB
#undef VF
#undef PW
#undef PVM
#undef SUM
#undef SB2
}

__device__ __forceinline__ void wait_count(unsigned* c, unsigned want) {
  unsigned spins = 0;
  while ((unsigned)__builtin_amdgcn_readfirstlane(__hip_atomic_load(c, __ATOMIC_RELAXED, __HIP_MEMORY_SCOPE_AGENT)) < want) {
    __builtin_amdgcn_s_sleep(4);
    if (++spins > (1u << 21)) break;
  }
  asm volatile("" ::: "memory");
}

__global__ __launch_bounds__(512, 2) void k2_fused(const u16* __restrict__ wd, const u16* __restrict__ wo, char* __restrict__ ws,
                                                   u16* __restrict__ out) {
  extern __shared__ __attribute__((aligned(16))) char lds_[];
  char LAS* lds = (char LAS*)lds_;
  const unsigned lds0 = (unsigned)(uintptr_t)lds;
  const int tid = threadIdx.x, lane = tid & 63;
  const int wid = __builtin_amdgcn_readfirstlane(tid >> 6);
  const int bid = blockIdx.x;
  const int xcd = bid & 7, idx = bid >> 3;
  const int sp = xcd >> 1, tn = idx & 7, tm = (xcd & 1) * 4 + (idx >> 3);
  const int mp = sp >> 1, np = sp & 1;
  const int tile = tm * 8 + tn;
  const int head = sp * 8 + tn, rb = tm;
  const int q0 = rb * 256;
  const char* qkv = ws + WS_QKV;
  unsigned* cntA = (unsigned*)(ws + WS_CNT);
  unsigned* cntP = (unsigned*)(ws + WS_CNT + 16 * 128);
  STAMP_DECL;
  STAMP(1, 0);

  const int qrow = q0 + wid * 32 + (lane & 31);
  const int rowlo = (rb > 0 ? q0 - 256 : 0) + wid * 32, rowhi = q0 + wid * 32;
  bf16x8 qf[2];
  float gval;
  {
    const char* qp = qkv + ((size_t)(head * 2) * S_ + q0 + wid * 32) * 32;
    const unsigned voffQ = (lane & 31) * 32 + (lane >> 5) * 16;
    qf[0] = *(const bf16x8*)(qp + voffQ);
    qf[1] = *(const bf16x8*)(qp + S_ * 32 + voffQ);
    gval = ((const float*)(ws + WS_G))[qrow];
    const unsigned voffK = (lane >> 1) * 32 + (((lane & 1) ^ ((lane >> 4) & 1)) << 4);

#pragma unroll
    for (int dh = 0; dh < 2; ++dh) {
      glds16(qkv + ((size_t)(64 + head * 2 + dh) * S_ + rowhi) * 32, voffK, lds0 + AT_K + dh * 16384 + (wid + 8) * 1024);
      glds16(qkv + ((size_t)(64 + head * 2 + dh) * S_ + rowlo) * 32, voffK, lds0 + AT_K + dh * 16384 + wid * 1024);
    }
  }

  const int ld = wid & 3;
  unsigned voffA, voffB0, voffB1m;
  auto set_voff = [&]() {
    const int l_ = fresh_lane();
    const int r = l_ >> 3, j = l_ & 7, q = r & 3;
    const int f0 = (q >> 1) & 1, f1 = f0 | 2;
    voffA = l_ * 16;
    voffB0 = r * 2048 + ((((j >> 1) ^ f0) << 1 | (j & 1)) << 4);
    voffB1m = (r + 8) * 2048 + ((((j >> 1) ^ f1) << 1 | (j & 1)) << 4) - 1024;
  };
  const char* hA = ws + WS_A2 + ((size_t)(sp * NH * 2 + (ld >> 1)) * S_ + tm * 256 + ((ld & 1) ^ mp) * 128) * 32;
  const char* hB = (const char*)wd + (size_t)(sp * NH * 32 + 16 * (ld & 1)) * 2048 + (size_t)(tn * 128 + ((ld >> 1) ^ np) * 64) * 2;
  const char* oA = ws + WS_A2 + ((size_t)(224 + sp * NO * 2 + (ld >> 1)) * S_ + tm * 256 + ((ld & 1) ^ mp) * 128) * 32;
  const char* oB = (const char*)wo + (size_t)(sp * NO * 32 + 16 * (ld & 1)) * 2048 + (size_t)(tn * 128 + ((ld >> 1) ^ np) * 64) * 2;
  const char* cA = ws + WS_A2 + ((size_t)(224 + 8 * NO + ld) * S_ + tm * 256 + mp * 128) * 32;
  const char* cB = (const char*)wo + (size_t)(128 * NO + 16 * ld) * 2048 + (size_t)(tn * 128 + np * 64) * 2;
  auto issue = [&](int i) {
    const unsigned dst = lds0 + (i % 6) * K2_SLOT;
    if (i < NH) {
      glds16x4<0>(hA + (size_t)i * (2 * S_ * 32), voffA, dst + ld * 4096);
      glds16x2(hB + (size_t)i * (32 * 2048), voffB0, voffB1m, dst + 16384 + ld * 2048);
    } else if (i < NK) {
      glds16x4<1>(oA + (size_t)(i - NH) * (2 * S_ * 32), voffA, dst + ld * 4096);
      glds16x2(oB + (size_t)(i - NH) * (32 * 2048), voffB0, voffB1m, dst + 16384 + ld * 2048);
    } else {
      glds16x4<1>(cA + (size_t)(i - NK) * (4 * S_ * 32), voffA, dst + ld * 4096);
      glds16x2(cB + (size_t)(i - NK) * (64 * 2048), voffB0, voffB1m, dst + 16384 + ld * 2048);
    }
  };

  {
    set_voff();
    const int hf = wid >> 2;
    unsigned voffBp = hf ? voffB1m + 1024 : voffB0;
    const char* pA = hA + hf * 2048;
    const unsigned dA = lds0 + ld * 4096 + hf * 2048, dB = lds0 + 16384 + ld * 2048 + hf * 1024;
    auto kwait = [&]() {
      asm volatile("" : "+v"(voffA), "+v"(voffBp));
      asm volatile("s_waitcnt vmcnt(0)" : "+v"(qf[0]), "+v"(qf[1]), "+v"(gval) :: "memory");
      BAR;
      asm volatile("" ::: "memory");
      STAMP(1, 1);
    };
    auto vload = [&]() {
#pragma unroll
      for (int dh = 0; dh < 2; ++dh) {
        glds16(qkv + ((size_t)(128 + head * 2 + dh) * S_ + rowlo) * 32, voffA, lds0 + (dh ? AT_V1 : AT_V) + wid * 1024);
        glds16(qkv + ((size_t)(128 + head * 2 + dh) * S_ + rowhi) * 32, voffA, lds0 + (dh ? AT_V1 : AT_V) + (wid + 8) * 1024);
      }
      if (lane < 32) *(LAS float*)(lds + K2_G + (wid * 32 + lane) * 4) = 1.0f - gval;
    };
    auto vwait = [&]() { WAIT_V(0); BAR; asm volatile("" ::: "memory"); };
    auto pf = [&](int kb) {
      const int st = kb / 3, j = kb % 3;
      if (j < 2) glds16(pA + (size_t)st * (2 * S_ * 32) + j * 1024, voffA, dA + st * K2_SLOT + j * 1024);
      else glds16(hB + (size_t)st * (32 * 2048), voffBp, dB + st * K2_SLOT);
    };
    if (rb == 0) attn_wave<true>(lds, ws, wid, lane, head, qrow, qf, gval, kwait, vload, vwait, pf);
    else attn_wave<false>(lds, ws, wid, lane, head, qrow, qf, gval, kwait, vload, vwait, pf);
  }
  STAMP(1, 2);

  if (wid >= 4) {
    set_voff();
    WAIT_V(8);
    WAIT_L0; BAR;
    issue(3); issue(4);
    WAIT_V(14);
    BAR;
    unsigned* ca = cntA + tm * 32;
    unsigned fl = 0;
    for (int P = 0; P < NS / 2; ++P) {
      const int a = 2 * P + 5;
      if (P == 9) asm volatile("global_load_dword %0, %1, off sc1" : "=v"(fl) : "v"(ca) : "memory");
      if (P == 11) {
        asm volatile("" : "+v"(fl));
        if ((unsigned)__builtin_amdgcn_readfirstlane(fl) < 256u) wait_count(ca, 256u);
      }
      if (a + 1 < NS) { issue(a); issue(a + 1); WAIT_V(12); }
      else if (a < NS) { issue(a); WAIT_V(6); }
      else { WAIT_V(0); }
      if (P == 0 && voffA == 0) __hip_atomic_fetch_add(ca, 1u, __ATOMIC_RELAXED, __HIP_MEMORY_SCOPE_AGENT);
      if (P + 1 < NS / 2) BAR;
    }
    STAMP(1, 3); STAMP(1, 4); STAMP(1, 5); STAMP(1, 6);
    STAMP_END(1, 7);
    return;
  }

  const int wm = wid & 1, wn = wid >> 1;
  const int fr = lane & 15, g4 = lane >> 4;
  const unsigned rAh = lds0 + (g4 >> 1) * 8192 + (wm * 64 + fr) * 32 + (g4 & 1) * 16;
  unsigned rAc = 0;
  unsigned rB0, rB1;
  { const int q = (lane & 15) >> 2, p = lane & 3;
    const int f = ((q >> 1) & 1) | ((g4 & 1) << 1);
    rB0 = lds0 + 16384 + g4 * 1024 + q * 128 + (((wn * 2) ^ f) << 5) + p * 8;
    rB1 = rB0 ^ 32u; }

  f32x4 acc[8][4];
#pragma unroll
  for (int m = 0; m < 8; ++m)
#pragma unroll
    for (int n = 0; n < 4; ++n) acc[m][n] = (f32x4){0.f, 0.f, 0.f, 0.f};
  bf16x8 A0[8], A1[8];
  s16x4 L0[4], H0[4], L1[4], H1[4];
#define RD(AF, BL, BH, k, so, COV) do { \
    if ((k) < 8) { const int n_ = (k) >> 1; const unsigned b_ = ((n_ & 1) ? rB1 : rB0) + (so); \
      if ((k) & 1) BH[n_] = lds_tr(LP(b_) + (n_ >> 1) * 4096 + 512); else BL[n_] = lds_tr(LP(b_) + (n_ >> 1) * 4096); } \
    else { const int m_ = (k) - 8; \
      if (COV) AF[m_] = lds_b128(LP(rAc + (so)) + (m_ >> 2) * 8192 + (m_ & 3) * 512); \
      else AF[m_] = lds_b128(LP(rAh + (so)) + (m_ >> 2) * 4096 + (m_ & 3) * 512); } } while (0)
#define MFH(AF, BL, BH, k) acc[(k) >> 2][(k) & 3] = __builtin_amdgcn_mfma_f32_16x16x32_bf16(cat8(BL[(k) & 3], BH[(k) & 3]), AF[(k) >> 2], acc[(k) >> 2][(k) & 3], 0, 0, 0)
#define STAGE_H(AC, LC, HC, AN, LN, HN, so, COV) do { \
    _Pragma("unroll") for (int k = 0; k < 32; ++k) { MFH(AC, LC, HC, k); if (k >= 1 && k < 17) RD(AN, LN, HN, k - 1, so, COV); SCHED; } } while (0)

  WAIT_V(8); WAIT_L0; BAR;
  asm volatile("" ::: "memory");
#pragma unroll
  for (int k = 0; k < 16; ++k) RD(A0, L0, H0, k, 0, 0);
  WAIT_V(2); WAIT_L0; BAR;
  SCHED;
  STAMP(1, 3);
  int s1 = 1;
#pragma unroll 1
  for (int ph = 0; ph < 2; ++ph) {
    const int np_ = ph ? (NO > 0 ? NO / 2 - 1 : 0) : (NO > 0 ? NH / 2 : NH / 2 - 1);
#pragma unroll 1
    for (int P = 0; P < np_; ++P) {
      const int s2 = (s1 == 5) ? 0 : s1 + 1;
      STAGE_H(A0, L0, H0, A1, L1, H1, s1 * K2_SLOT, 0);
      STAGE_H(A1, L1, H1, A0, L0, H0, s2 * K2_SLOT, 0);
      s1 = s2 + 1;
      if (P == 2) { WAIT_V(0); if (fresh_lane() == 0) __hip_atomic_fetch_add(cntA + tm * 32, 1u, __ATOMIC_RELAXED, __HIP_MEMORY_SCOPE_AGENT); }
      WAIT_L0; BAR; SCHED;
    }
    if (ph == 0 && NO > 0) {
      const int l4 = fresh_lane();
#pragma unroll
      for (int m = 0; m < 8; ++m) {
        const float sc = *(const LAS float*)(lds + K2_G + (((m >> 2) ^ mp) * 128 + wm * 64 + (m & 3) * 16 + (l4 & 15)) * 4);
#pragma unroll
        for (int n = 0; n < 4; ++n) acc[m][n] *= sc;
      }
      SCHED;
    }
  }
  STAGE_H(A0, L0, H0, A1, L1, H1, ((NK - 1) % 6) * K2_SLOT, 0);
  { const int l2 = fresh_lane(); rAc = lds0 + (l2 >> 5) * 4096 + (wm * 64 + (l2 & 15)) * 32 + ((l2 >> 4) & 1) * 16; }
  STAGE_H(A1, L1, H1, A0, L0, H0, (NK % 6) * K2_SLOT, 1);
  WAIT_L0; BAR; SCHED;
  STAMP(1, 4);

  auto rs = __builtin_amdgcn_make_buffer_rsrc((void*)(ws + WS_PART), 0, 16u << 20, 0x00020000);
  const int l3 = fresh_lane();
  if (NO == 0) {
#pragma unroll
    for (int m = 0; m < 8; ++m) {
      const float sc = *(const LAS float*)(lds + K2_G + (((m >> 2) ^ mp) * 128 + wm * 64 + (m & 3) * 16 + (l3 & 15)) * 4);
#pragma unroll
      for (int n = 0; n < 4; ++n) acc[m][n] *= sc;
    }
  }
#pragma unroll
  for (int d = 3; d >= 1; --d) {
    const int h = d >> 1, c = d & 1;
#pragma unroll
    for (int mb = 0; mb < 4; ++mb) {
      const f32x4 u = acc[4 * h + mb][2 * c], v = acc[4 * h + mb][2 * c + 1];
      u32x4 w = {cvtpk(u[0], u[1]), cvtpk(u[2], u[3]), cvtpk(v[0], v[1]), cvtpk(v[2], v[3])};
      const unsigned off = (((((unsigned)tile * 4 + (d ^ sp)) * 4 + sp) * 4 + wid) * 4 + mb) * 1024u + l3 * 16;
      __builtin_amdgcn_raw_buffer_store_b128(w, rs, off, 0, 16);
    }
  }
  f32x4 acK[4][2];
#pragma unroll
  for (int mb = 0; mb < 4; ++mb)
#pragma unroll
    for (int j = 0; j < 2; ++j) acK[mb][j] = acc[mb][j];
  SCHED;
#define MFC(AF, BL, BH, k) acK[((k) >> 1) & 3][(k) & 1] = __builtin_amdgcn_mfma_f32_16x16x32_bf16(cat8(BL[2 * ((k) >> 3) + ((k) & 1)], BH[2 * ((k) >> 3) + ((k) & 1)]), \
                                                             AF[4 * ((k) >> 3) + (((k) >> 1) & 3)], acK[((k) >> 1) & 3][(k) & 1], 0, 0, 0)
#define STAGE_C(AC, LC, HC, AN, LN, HN, so) do { \
    _Pragma("unroll") for (int k = 0; k < 16; ++k) { MFC(AC, LC, HC, k); RD(AN, LN, HN, k, so, 1); SCHED; } } while (0)
  u32x4 part[3][4];
  unsigned* myc = cntP + (tile * 4 + wid) * 32;
  unsigned flagv = 0;
#define XSIG 2
#define XLD 2
#pragma unroll
  for (int P = NK / 2; P < NS / 2; ++P) {
    STAGE_C(A0, L0, H0, A1, L1, H1, ((2 * P + 1) % 6) * K2_SLOT);
    if (P + 1 < NS / 2) { STAGE_C(A1, L1, H1, A0, L0, H0, ((2 * P + 2) % 6) * K2_SLOT); }
    else {
#pragma unroll
      for (int k = 0; k < 16; ++k) MFC(A1, L1, H1, k);
    }
    if (P == NK / 2 + XSIG) { WAIT_V(0); if (l3 == 0) __hip_atomic_fetch_add(myc, 1u, __ATOMIC_RELAXED, __HIP_MEMORY_SCOPE_AGENT); }
    if (P == NS / 2 - 2 - XLD) flagv = __hip_atomic_load(myc, __ATOMIC_RELAXED, __HIP_MEMORY_SCOPE_AGENT);
    if (P + 1 < NS / 2) { WAIT_L0; BAR; SCHED; }
    if (P == NS / 2 - 1 - XLD) {
      if ((unsigned)__builtin_amdgcn_readfirstlane(flagv) < 4u) wait_count(myc, 4u);
      asm volatile("" ::: "memory");
#pragma unroll
      for (int jj = 0; jj < 3; ++jj) {
        const int src = (sp + 1 + jj) & 3;
#pragma unroll
        for (int mb = 0; mb < 4; ++mb) {
          const unsigned off = (((((unsigned)tile * 4 + sp) * 4 + src) * 4 + wid) * 4 + mb) * 1024u + l3 * 16;
          part[jj][mb] = __builtin_amdgcn_raw_buffer_load_b128(rs, off, 0, 16);
        }
      }
    }
  }
  STAMP(1, 5);
  const int lane_e = fresh_lane();
#pragma unroll
  for (int mb = 0; mb < 4; ++mb) {
    f32x4 u = acK[mb][0], v = acK[mb][1];
#pragma unroll
    for (int jj = 0; jj < 3; ++jj) {
      const u32x4 w = part[jj][mb];
      u[0] += __uint_as_float(w[0] << 16); u[1] += __uint_as_float(w[0] & 0xffff0000u);
      u[2] += __uint_as_float(w[1] << 16); u[3] += __uint_as_float(w[1] & 0xffff0000u);
      v[0] += __uint_as_float(w[2] << 16); v[1] += __uint_as_float(w[2] & 0xffff0000u);
      v[2] += __uint_as_float(w[3] << 16); v[3] += __uint_as_float(w[3] & 0xffff0000u);
    }
    auto r0 = __builtin_amdgcn_permlane16_swap(cvtpk(u[0], u[1]), cvtpk(v[0], v[1]), false, false);
    auto r1 = __builtin_amdgcn_permlane16_swap(cvtpk(u[2], u[3]), cvtpk(v[2], v[3]), false, false);
    u32x4 w = {r0[0], r1[0], r0[1], r1[1]};
    const int row = tm * 256 + mp * 128 + wm * 64 + mb * 16 + (lane_e & 15);
    const int col = tn * 128 + np * 64 + wn * 32 + ((lane_e >> 4) & 1) * 16 + (lane_e >> 5) * 8;
    *(u32x4*)(out + (size_t)row * D_ + col) = w;
  }
  STAMP(1, 6);
  STAMP_END(1, 7);
}

constexpr int G1_LDS_A = 0;
constexpr int G1_LDS_B = 65536;
constexpr int G1_LDS_B4 = 131072;
constexpr int G1_LDS_GW = 147456;
constexpr int G1_LDS_DUMMY = G1_LDS_GW + 2048;
constexpr int G1_LDS = G1_LDS_DUMMY + 2048;

#define QMODE 2
#define HMODE 2
#define TBACK 0
template <int MODE, int OFF>
__device__ __forceinline__ void store_pair16(const void* sbase, unsigned voff, unsigned x0, unsigned x1, unsigned y0, unsigned y1) {
  auto r0 = __builtin_amdgcn_permlane16_swap(x0, y0, false, false);
  auto r1 = __builtin_amdgcn_permlane16_swap(x1, y1, false, false);
  u32x4 w = {r0[0], r1[0], r0[1], r1[1]};
  if (MODE == 0) asm volatile("global_store_dwordx4 %0, %1, %2 offset:%3\n\ts_nop 1" ::"v"(voff), "v"(w), "s"(sbase), "n"(OFF) : "memory");
  else if (MODE == 1) asm volatile("global_store_dwordx4 %0, %1, %2 offset:%3 nt\n\ts_nop 1" ::"v"(voff), "v"(w), "s"(sbase), "n"(OFF) : "memory");
  else asm volatile("global_store_dwordx4 %0, %1, %2 offset:%3 sc1\n\ts_nop 1" ::"v"(voff), "v"(w), "s"(sbase), "n"(OFF) : "memory");
}

__global__ __launch_bounds__(512, 2) void k_gemm1(const u16* __restrict__ x, const u16* __restrict__ wqkv, const u16* __restrict__ wg,
                                                  const u16* __restrict__ wu, const u16* __restrict__ gate_w, char* __restrict__ ws,
                                                  const u16* __restrict__ wd, const u16* __restrict__ wo) {
  extern __shared__ __attribute__((aligned(16))) char lds_[];
  char LAS* lds = (char LAS*)lds_;
  const unsigned lds0 = (unsigned)(uintptr_t)lds;
  const int tid = threadIdx.x, lane = tid & 63;
  const int wid = __builtin_amdgcn_readfirstlane(tid >> 6);
  const int wr = wid >> 2, wc = wid & 3;
  const int bid = blockIdx.x;
  const int xcd = bid & 7, idx = bid >> 3;
  const int sp = xcd >> 1, tm = (xcd & 1) * 4 + (idx >> 3), ct = idx & 7;
  const int tn = sp * 8 + ct;

  int ptype[5], pcol[5];
  {
    auto qc = [&](int i) { return (i >> 2) * 1024 + 256 * sp + 64 * (i & 3); };
    if (ct < 6) {
      ptype[0] = 1; ptype[1] = 2; pcol[0] = pcol[1] = 896 * sp + 128 * ct;
      ptype[2] = 1; ptype[3] = 2; pcol[2] = pcol[3] = 896 * sp + 128 * ct + 64;
      ptype[4] = 0; pcol[4] = qc(ct);
    } else {
      ptype[0] = 1; ptype[1] = 2; pcol[0] = pcol[1] = 896 * sp + 768 + 64 * (ct - 6);
      ptype[2] = ptype[3] = ptype[4] = 0;
      pcol[2] = qc(6 + 3 * (ct - 6)); pcol[3] = qc(7 + 3 * (ct - 6)); pcol[4] = qc(8 + 3 * (ct - 6));
    }
  }

  const char* sA = (const char*)x + (size_t)(tm * 256 + wid * 8) * 2048;
  unsigned voffA;
  { const int r = lane >> 3, j = lane & 7; voffA = r * 2048 + ((j ^ ((4 * (wid & 1) + (r >> 1)) & 7)) << 4); }
  const unsigned ldsA = lds0 + G1_LDS_A + wid * 1024;
  const int myp = wid >> 1;
  int mt = ptype[0], mc = pcol[0];
#pragma unroll
  for (int p = 1; p < 4; ++p) if (myp == p) { mt = ptype[p]; mc = pcol[p]; }
  const unsigned strB = (mt == 0) ? 6144u : 7168u;
  const char* sB = (const char*)(mt == 0 ? wqkv : (mt == 1 ? wg : wu)) + (size_t)mc * 2 + (size_t)(16 * (wid & 1)) * strB;
  unsigned voffB0, voffB1;
  { const int r = lane >> 3, j = lane & 7, q = r & 3;
    const int f0 = (q >> 1) & 1, f1 = f0 | 2;
    voffB0 = r * strB + ((((j >> 1) ^ f0) << 1 | (j & 1)) << 4);
    voffB1 = (r + 8) * strB + ((((j >> 1) ^ f1) << 1 | (j & 1)) << 4); }
  const unsigned ldsB = lds0 + G1_LDS_B + myp * 8192 + (wid & 1) * 2048;
  constexpr unsigned strB4 = 6144u;
  const char* sB4 = (const char*)wqkv + (size_t)pcol[4] * 2 + (size_t)(8 * wid) * strB4;
  unsigned voffB4;
  { const int r = lane >> 3, j = lane & 7, q = r & 3;
    const int f = ((q >> 1) & 1) | ((wid & 1) << 1);
    voffB4 = r * strB4 + ((((j >> 1) ^ f) << 1 | (j & 1)) << 4); }
  const unsigned ldsB4 = lds0 + G1_LDS_B4 + wid * 1024;

#define KC(kt) ((unsigned)((kt) < 15 ? (kt) : 15))
#define G1_A0(kt, b) do { const char* _p = sA + KC(kt) * 128u; glds16(_p, voffA, ldsA + (b) * 32768); \
                          glds16(_p + 64 * 2048, voffA, ldsA + (b) * 32768 + 8192); } while (0)
#define G1_A1(kt, b) do { const char* _p = sA + KC(kt) * 128u; glds16(_p + 128 * 2048, voffA, ldsA + (b) * 32768 + 16384); \
                          glds16(_p + 192 * 2048, voffA, ldsA + (b) * 32768 + 24576); } while (0)
#define G1_X(kt, b) do { const char* _p = sB + KC(kt) * 64u * strB; glds16(_p, voffB0, ldsB + (b) * 32768); \
                         glds16(_p, voffB1, ldsB + (b) * 32768 + 1024); } while (0)
#define G1_Y(kt, b) do { const char* _p = sB + (KC(kt) * 64u + 32u) * strB; glds16(_p, voffB0, ldsB + (b) * 32768 + 4096); \
                         glds16(_p, voffB1, ldsB + (b) * 32768 + 4096 + 1024); } while (0)
#define G1_Z(kt, b) glds16(sB4 + KC(kt) * 64u * strB4, voffB4, ldsB4 + (b) * 8192)

  STAMP_DECL;
  STAMP(0, 0);
  glds4((const char*)gate_w + wid * 256, lane * 4, lds0 + G1_LDS_GW + wid * 256);
  G1_X(0, 0); G1_A0(0, 0); G1_A1(0, 0); G1_Z(0, 0); G1_Y(0, 0); G1_X(1, 1);
#define TOUCH 1
#define TD 5
#define TPRO 3
#define CODEPF 1
  const char* tp; unsigned voffT, tstep, tlead, tmax = 15;
  if (wid == 0) { tp = (const char*)x + (size_t)(tm * 256 + ct * 32) * 2048; voffT = (lane & 31) * 2048; tstep = 128; tlead = TD; }
  else if (wid <= 5) {
    int t_ = ptype[0], c_ = pcol[0];
#pragma unroll
    for (int p = 1; p < 5; ++p) if (wid - 1 == p) { t_ = ptype[p]; c_ = pcol[p]; }
    const unsigned st_ = (t_ == 0) ? 6144u : 7168u;
    tp = (const char*)(t_ == 0 ? wqkv : (t_ == 1 ? wg : wu)) + (size_t)c_ * 2 + (size_t)((tm & 3) * 16) * st_;
    voffT = (lane & 15) * st_; tstep = 64 * st_; tlead = TD;
  } else if (wid == 6) { tp = (const char*)wd + (size_t)bid * 28672; voffT = (lane % 14) * 128; tstep = 1792; tlead = 1; }
  else if (idx >= 16 || !CODEPF) { tp = (const char*)wo + (size_t)(xcd * 16 + (idx & 15)) * 16384; voffT = (lane & 7) * 128; tstep = 1024; tlead = 1; }
  else {
    unsigned long long pc; int d;
    asm volatile("s_getpc_b64 %0\n\ts_mov_b32 %1, _Z8k2_fusedPKtS0_PcPt@rel32@lo+4" : "=s"(pc), "=s"(d));
    const bool ok = d < -4096;
    const unsigned n4k = ok ? (unsigned)(-d) / 4096u : 1u;
    tp = ok ? (const char*)(pc + (long long)d) : (const char*)wo;
    voffT = (lane & 31) * 128; tstep = 4096; tlead = (unsigned)-8; tmax = (n4k < 7u ? n4k : 7u) - 1u;
  }
  auto touch = [&](int j) { const unsigned k_ = (unsigned)j + tlead; glds4(tp + (k_ < tmax ? k_ : tmax) * tstep, voffT, lds0 + G1_LDS_DUMMY + wid * 256); };

  const int fr = lane & 15, g4 = lane >> 4;
  const char LAS* rA0 = lds + G1_LDS_A + wr * 8192 + fr * 128 + (((0 + g4) ^ (fr >> 1)) << 4);
  const char LAS* rA1 = lds + G1_LDS_A + wr * 8192 + fr * 128 + (((4 + g4) ^ (fr >> 1)) << 4);
  const char LAS* rB; const char LAS* rB4;
  { const int q = (lane & 15) >> 2, p = lane & 3;
    const int f = ((q >> 1) & 1) | ((g4 & 1) << 1);
    const int o = g4 * 1024 + q * 128 + ((wc ^ f) << 5) + p * 8;
    rB = lds + G1_LDS_B + o;
    rB4 = lds + G1_LDS_B4 + o; }

  const char LAS* rG; const char LAS* rGW;
  { const int r = tn * 8 + wid, l = lane & 31;
    rG = lds + G1_LDS_A + r * 128 + (((l >> 2) ^ ((r >> 1) & 7)) << 4) + (l & 3) * 4;
    rGW = lds + G1_LDS_GW + l * 4; }
  float gsum = 0.f;
  f32x4 acc[2][4][5];
#pragma unroll
  for (int a = 0; a < 2; ++a)
#pragma unroll
    for (int m = 0; m < 4; ++m)
#pragma unroll
      for (int p = 0; p < 5; ++p) acc[a][m][p] = (f32x4){0.f, 0.f, 0.f, 0.f};
  bf16x8 Af[8], Bf[5];

#define G1_LDB(b, ks) do { _Pragma("unroll") for (int p = 0; p < 4; ++p) { \
      s16x4 lo = lds_tr(rB + (b) * 32768 + p * 8192 + (ks) * 4096), hi = lds_tr(rB + (b) * 32768 + p * 8192 + (ks) * 4096 + 512); Bf[p] = cat8(lo, hi); } \
    { s16x4 lo = lds_tr(rB4 + (b) * 8192 + (ks) * 4096), hi = lds_tr(rB4 + (b) * 8192 + (ks) * 4096 + 512); Bf[4] = cat8(lo, hi); } } while (0)
#define G1_LDA(b, ks) do { _Pragma("unroll") for (int m = 0; m < 8; ++m) \
      Af[m] = lds_b128(((ks) ? rA1 : rA0) + (b) * 32768 + (m >> 2) * 16384 + (m & 3) * 2048); } while (0)
#define EARLYB 0
#define G1_MMA() do { __builtin_amdgcn_s_setprio(1); _Pragma("unroll") for (int k = 0; k < 40; ++k) { const int m = k / 5, p = k % 5; \
      if (k == 40 - EARLYB) { SCHED; __builtin_amdgcn_s_setprio(3); BAR; SCHED; } \
      acc[m >> 2][m & 3][p] = __builtin_amdgcn_mfma_f32_16x16x32_bf16(Bf[p], Af[m], acc[m >> 2][m & 3][p], 0, 0, 0); } \
      __builtin_amdgcn_s_setprio(0); if (EARLYB == 0) BAR; } while (0)
#define WV_A WAIT_V(5)
#define WV_B WAIT_V(10)
  WAIT_V(4); BAR;
  STAMP(0, 1);
  if (TOUCH) {
#pragma unroll
    for (int e = TPRO; e >= 1; --e) touch(-e);
  }
  if (wr == 1) BAR;

#define G1_TILE(t, b) do { \
    G1_LDB(b, 0); G1_LDA(b, 0); \
    G1_A0((t) + 1, (b) ^ 1); G1_A1((t) + 1, (b) ^ 1); G1_Z((t) + 1, (b) ^ 1); G1_Y((t) + 1, (b) ^ 1); \
    WV_B; WAIT_L0; BAR; G1_MMA(); SCHED; \
    G1_LDB(b, 1); G1_LDA(b, 1); \
    { const unsigned xa = *(const LAS unsigned*)(rG + (b) * 32768), wa = *(const LAS unsigned*)(rGW + (t) * 128); \
      G1_X((t) + 2, b); if (TOUCH) touch(t); \
      WAIT_L0; \
      gsum = __builtin_fmaf(__uint_as_float(xa << 16), __uint_as_float(wa << 16), gsum); \
      gsum = __builtin_fmaf(__uint_as_float(xa & 0xffff0000u), __uint_as_float(wa & 0xffff0000u), gsum); \
      asm volatile("" : "+v"(gsum)); } \
    WV_A; BAR; G1_MMA(); SCHED; \
  } while (0)

#pragma unroll 1
  for (int t = 0; t < 16; t += 2) {
    G1_TILE(t, 0);
    G1_TILE(t + 1, 1);
  }
  STAMP(0, 2);
  const int lane_e = fresh_lane();
#define lane lane_e

  {
#pragma unroll
    for (int o = 16; o >= 1; o >>= 1) gsum += __shfl_xor(gsum, o);
    if (lane == 0) ((float*)(ws + WS_G))[tm * 256 + tn * 8 + wid] = 1.0f / (1.0f + __expf(-gsum));
    if (wid == 0 && lane == 0) ((unsigned*)(ws + WS_CNT))[(16 + bid) * 32] = 0u;
    if (wid == 1 && lane == 0 && bid < 16) ((unsigned*)(ws + WS_CNT))[bid * 32] = 0u;
  }

  const unsigned voffS = (((lane >> 4) & 1) * 16 + (lane & 15)) * 32 + (lane >> 5) * 16;
  const size_t rowoff = (size_t)(tm * 256 + wr * 64) * 32 + 2048;
#define G1_STQ(p) do { const float sc = pcol[p] < D_ ? QSCALE : 1.0f; \
    const char* dst = ws + WS_QKV + (size_t)(pcol[p] / 16 + wc) * (S_ * 32) + rowoff; \
    { const f32x4 u = acc[0][0][p] * sc, v = acc[0][1][p] * sc; store_pair16<QMODE, -2048>(dst, voffS, cvtpk(u[0], u[1]), cvtpk(u[2], u[3]), cvtpk(v[0], v[1]), cvtpk(v[2], v[3])); } \
    { const f32x4 u = acc[0][2][p] * sc, v = acc[0][3][p] * sc; store_pair16<QMODE, -1024>(dst, voffS, cvtpk(u[0], u[1]), cvtpk(u[2], u[3]), cvtpk(v[0], v[1]), cvtpk(v[2], v[3])); } \
    { const f32x4 u = acc[1][0][p] * sc, v = acc[1][1][p] * sc; store_pair16<QMODE, 2048>(dst, voffS, cvtpk(u[0], u[1]), cvtpk(u[2], u[3]), cvtpk(v[0], v[1]), cvtpk(v[2], v[3])); } \
    { const f32x4 u = acc[1][2][p] * sc, v = acc[1][3][p] * sc; store_pair16<QMODE, 3072>(dst, voffS, cvtpk(u[0], u[1]), cvtpk(u[2], u[3]), cvtpk(v[0], v[1]), cvtpk(v[2], v[3])); } } while (0)
#define G1_H1(pp, a, m, OFF) do { const f32x4 g0 = acc[a][m][2 * (pp)], u0 = acc[a][m][2 * (pp) + 1], g1 = acc[a][(m) + 1][2 * (pp)], u1 = acc[a][(m) + 1][2 * (pp) + 1]; \
    const f32x2 h0 = geglu2(g0.xy, u0.xy), h1 = geglu2(g0.zw, u0.zw), h2 = geglu2(g1.xy, u1.xy), h3 = geglu2(g1.zw, u1.zw); \
    store_pair16<HMODE, OFF>(dst, voffS, cvtpk(h0.x, h0.y), cvtpk(h1.x, h1.y), cvtpk(h2.x, h2.y), cvtpk(h3.x, h3.y)); } while (0)
#define G1_STH(pp) do { const char* dst = ws + WS_A2 + (size_t)(pcol[2 * (pp)] / 16 + wc) * (S_ * 32) + rowoff; \
    G1_H1(pp, 0, 0, -2048); G1_H1(pp, 0, 2, -1024); G1_H1(pp, 1, 0, 2048); G1_H1(pp, 1, 2, 3072); } while (0)
#define G1_TB(p) glds4(ws + WS_QKV + (size_t)(pcol[p] / 16 + wc) * (S_ * 32) + rowoff - 2048, voffTB, lds0 + G1_LDS_DUMMY + wid * 256)
  const unsigned voffTB = ((lane >> 4) & 1) * 4096 + (lane & 15) * 128;
  G1_STQ(4);
  if (wr == 0) BAR;
  WAIT_V(4);
  if (ptype[2] == 0) {
    G1_STQ(2); G1_STQ(3);
    G1_STH(0);
    if (TBACK) { WAIT_V(4); G1_TB(4); G1_TB(2); G1_TB(3); }
  } else {
    G1_STH(0);
    if (TBACK) { WAIT_V(4); G1_TB(4); }
    G1_STH(1);
  }
#undef lane
  STAMP(0, 3);
  STAMP(0, 4);
  STAMP_END(0, 5);
}

extern "C" void kernel_launch(void* const* d_in, const int* in_sizes, int n_in, void* d_out, int out_size, void* d_ws, size_t ws_size,
                              hipStream_t stream) {
  const u16* x = (const u16*)d_in[0];
  const u16* wqkv = (const u16*)d_in[1];
  const u16* wo = (const u16*)d_in[2];
  const u16* wg = (const u16*)d_in[4];
  const u16* wu = (const u16*)d_in[5];
  const u16* wd = (const u16*)d_in[6];
  const u16* gate_w = (const u16*)d_in[7];
  char* ws = (char*)d_ws;
  (void)hipFuncSetAttribute((const void*)k_gemm1, hipFuncAttributeMaxDynamicSharedMemorySize, G1_LDS);
  (void)hipFuncSetAttribute((const void*)k2_fused, hipFuncAttributeMaxDynamicSharedMemorySize, K2_LDS);
  k_gemm1<<<256, 512, G1_LDS, stream>>>(x, wqkv, wg, wu, gate_w, ws, wd, wo);
  k2_fused<<<256, 512, K2_LDS, stream>>>(wd, wo, ws, (u16*)d_out);
}
```

```cpp
#include <hip/hip_runtime.h>
#include <hip/hip_bf16.h>
#include <stdint.h>

#define LAS __attribute__((address_space(3)))
typedef unsigned short u16;
typedef short bf16x8 __attribute__((ext_vector_type(8)));
typedef short s16x4 __attribute__((ext_vector_type(4)));
typedef float f32x4 __attribute__((ext_vector_type(4)));
typedef float f32x16 __attribute__((ext_vector_type(16)));
typedef unsigned u32x4 __attribute__((ext_vector_type(4)));
typedef unsigned u32x2 __attribute__((ext_vector_type(2)));

constexpr int S_ = 2048, D_ = 1024, F_ = 3584, NQKV = 3072, K2 = F_ + D_;
constexpr float QSCALE = 0.17677669529663687f * 1.4426950408889634f;

constexpr size_t WS_QKV = 0;
constexpr size_t WS_A2 = WS_QKV + (size_t)NQKV * S_ * 2;
constexpr size_t WS_G = WS_A2 + (size_t)K2 * S_ * 2;
constexpr size_t WS_CNT = WS_G + S_ * 4;
constexpr size_t WS_PART = WS_CNT + 65536;

#define WAIT_V(n) asm volatile("s_waitcnt vmcnt(" #n ")" ::: "memory")
#define WAIT_L0 asm volatile("s_waitcnt lgkmcnt(0)" ::: "memory")
#define BAR __builtin_amdgcn_s_barrier()
#define SCHED __builtin_amdgcn_sched_barrier(0)

#define STAMP_DECL do {} while (0)
#define STAMP(kid, i) do {} while (0)
#define STAMP_END(kid, n) do {} while (0)
__device__ __forceinline__ int fresh_lane() {
  int l;
  asm volatile("v_mbcnt_lo_u32_b32 %0, -1, 0\n\tv_mbcnt_hi_u32_b32 %0, -1, %0" : "=v"(l));
  return l;
}
__device__ __forceinline__ void glds16(const void* sbase, unsigned voff, unsigned lds_addr) {
  asm volatile("s_mov_b32 m0, %2\n\ts_nop 0\n\tglobal_load_lds_dwordx4 %0, %1" ::"v"(voff), "s"(sbase), "s"(lds_addr) : "memory");
}
typedef __bf16 bf16x2_t __attribute__((ext_vector_type(2)));
typedef float f32x2 __attribute__((ext_vector_type(2)));
__device__ __forceinline__ unsigned cvtpk(float lo, float hi) {
  return __builtin_bit_cast(unsigned, __builtin_convertvector((f32x2){lo, hi}, bf16x2_t));
}
__device__ __forceinline__ bf16x8 lds_b128(const char LAS* p) { return *(const LAS bf16x8*)p; }
__device__ __forceinline__ s16x4 lds_tr(const char LAS* p) {
  return __builtin_bit_cast(s16x4, __builtin_amdgcn_ds_read_tr16_b64_v4i16((LAS s16x4*)p));
}
__device__ __forceinline__ bf16x8 cat8(s16x4 a, s16x4 b) {
  return (bf16x8){a[0], a[1], a[2], a[3], b[0], b[1], b[2], b[3]};
}
__device__ __forceinline__ f32x2 geglu2(f32x2 x, f32x2 u) {
  const float c0 = -2.0f * 0.7978845608028654f * 1.4426950408889634f;
  const float c1 = c0 * 0.044715f;
  const f32x2 t = x * x;
  const f32x2 z = x * (t * c1 + c0);
  f32x2 e; e.x = __builtin_amdgcn_exp2f(z.x); e.y = __builtin_amdgcn_exp2f(z.y);
  const f32x2 d = e + 1.0f;
  f32x2 r; r.x = __builtin_amdgcn_rcpf(d.x); r.y = __builtin_amdgcn_rcpf(d.y);
  return (x * u) * r;
}
__device__ __forceinline__ void glds4(const void* sbase, unsigned voff, unsigned lds_addr) {
  asm volatile("s_mov_b32 m0, %2\n\ts_nop 0\n\tglobal_load_lds_dword %0, %1" ::"v"(voff), "s"(sbase), "s"(lds_addr) : "memory");
}

constexpr int K2_SLOT = 24576;
constexpr int K2_G = 6 * K2_SLOT;
constexpr int K2_LDS = K2_G + 1024;
constexpr int AT_K = 3 * K2_SLOT;
constexpr int AT_V = AT_K + 32768;
constexpr int AT_V1 = AT_V + 16384 + 128;
static_assert(AT_V1 + 16384 <= K2_LDS, "lds");
#define NO_ 0
constexpr int NH = 28, NO = NO_, NK = NH + NO, NC = 16 - 2 * NO, NS = NK + NC;

__device__ __forceinline__ void st16_sc1(void* p, u32x4 w) {
  asm volatile("global_store_dwordx4 %0, %1, off sc1\n\ts_nop 1" ::"v"(p), "v"(w) : "memory");
}
template <int SC1>
__device__ __forceinline__ void glds16x4(const void* sbase, unsigned voff, unsigned lds_addr) {
  if (SC1)
    asm volatile("s_mov_b32 m0, %2\n\ts_nop 0\n\tglobal_load_lds_dwordx4 %0, %1 sc1\n\tglobal_load_lds_dwordx4 %0, %1 offset:1024 sc1\n\t"
                 "global_load_lds_dwordx4 %0, %1 offset:2048 sc1\n\tglobal_load_lds_dwordx4 %0, %1 offset:3072 sc1" ::"v"(voff), "s"(sbase), "s"(lds_addr) : "memory");
  else
    asm volatile("s_mov_b32 m0, %2\n\ts_nop 0\n\tglobal_load_lds_dwordx4 %0, %1\n\tglobal_load_lds_dwordx4 %0, %1 offset:1024\n\t"
                 "global_load_lds_dwordx4 %0, %1 offset:2048\n\tglobal_load_lds_dwordx4 %0, %1 offset:3072" ::"v"(voff), "s"(sbase), "s"(lds_addr) : "memory");
}
__device__ __forceinline__ void glds16x2(const void* sbase, unsigned voff0, unsigned voff1m, unsigned lds_addr) {
  asm volatile("s_mov_b32 m0, %3\n\ts_nop 0\n\tglobal_load_lds_dwordx4 %0, %2\n\tglobal_load_lds_dwordx4 %1, %2 offset:1024" ::"v"(voff0), "v"(voff1m),
               "s"(sbase), "s"(lds_addr) : "memory");
}
#define LP(a) ((const char LAS*)(uintptr_t)(a))

__device__ __forceinline__ float vsub(float a, float b) { float r; asm("v_sub_f32 %0, %1, %2" : "=v"(r) : "v"(a), "v"(b)); return r; }
__device__ __forceinline__ float vmul(float a, float b) { float r; asm("v_mul_f32 %0, %1, %2" : "=v"(r) : "v"(a), "v"(b)); return r; }
template <bool FIRST, class KW, class VL, class VW, class PF>
__device__ __forceinline__ void attn_wave(const char LAS* lds, char* ws, int wid, int lane, int head, int qrow, const bf16x8 (&qf)[2], const float& gval,
                                          KW kwait, VL vload, VW vwait, PF pf) {
  const int r32 = lane & 31, hi = lane >> 5;
  const int kb0 = FIRST ? 8 - wid : 0;
  const int dq = r32 - 4 * hi;
  const char LAS* kbase = lds + AT_K + wid * 1024 + r32 * 32 + ((hi ^ ((r32 >> 3) & 1)) << 4);
  const f32x16 zero = {0.f, 0.f, 0.f, 0.f, 0.f, 0.f, 0.f, 0.f, 0.f, 0.f, 0.f, 0.f, 0.f, 0.f, 0.f, 0.f};
  bf16x8 kf[9][2];
  f32x16 sb[3];
#define QKA(kb, dst, basev) dst = __builtin_amdgcn_mfma_f32_32x32x16_bf16(kf[kb][0], qf[0], (kb) == 0 ? mk0 : ((kb) == 8 ? mk8 : basev), 0, 0, 0)
#define QKB(kb, dst) dst = __builtin_amdgcn_mfma_f32_32x32x16_bf16(kf[kb][1], qf[1], dst, 0, 0, 0)
#define MX3(a, b, c) __builtin_fmaxf(__builtin_fmaxf(a, b), c)
  f32x16 mk0, mk8;
#pragma unroll
  for (int r = 0; r < 16; ++r) { const bool ab = ((r & 3) + 8 * (r >> 2)) > dq; mk0[r] = ab ? 0.0f : -INFINITY; mk8[r] = ab ? -INFINITY : 0.0f; }
  bf16x8 sel;
  { const short one = (((lane >> 4) & 1) == (lane & 1)) ? (short)0x3F80 : (short)0;
    sel = (bf16x8){one, one, one, one, one, one, one, one}; }
  const char LAS* vbase;
  { const int g = lane >> 4, q = (lane & 15) >> 2, p = lane & 3;
    vbase = lds + ((g & 1) ? AT_V1 : AT_V) + wid * 1024 + (4 * (g >> 1) + q) * 32 + p * 8; }
  u16* optr = (u16*)(ws + WS_A2) + ((size_t)(224 + head * 2) * S_ + qrow) * 16 + hi * 8;
  asm volatile("" : "+v"(mk0), "+v"(mk8), "+v"(sel), "+v"(vbase), "+v"(kbase), "+v"(optr));
  kwait();
#pragma unroll
  for (int kb = 0; kb < 9; ++kb) { kf[kb][0] = lds_b128(kbase + kb * 1024); kf[kb][1] = lds_b128(kbase + 16384 + kb * 1024); }
  vload();
  float ma = -INFINITY, mb = -INFINITY;
  SCHED;
#pragma unroll
  for (int kb = 0; kb < 11; ++kb) {
    const int pb = kb - 2;
    float a_ = ma, b_ = mb;
    if (kb < 9) { QKA(kb, sb[kb % 3], zero); SCHED; }
    if (pb >= 0) { const f32x16& t_ = sb[pb % 3]; a_ = MX3(a_, t_[0], t_[1]); b_ = MX3(b_, t_[2], t_[3]); a_ = MX3(a_, t_[4], t_[5]); b_ = MX3(b_, t_[6], t_[7]); SCHED; }
    if (kb < 9) { QKB(kb, sb[kb % 3]); SCHED; }
    if (pb >= 0) { const f32x16& t_ = sb[pb % 3]; a_ = MX3(a_, t_[8], t_[9]); b_ = MX3(b_, t_[10], t_[11]); a_ = MX3(a_, t_[12], t_[13]); b_ = MX3(b_, t_[14], t_[15]);
      if (FIRST) { ma = pb >= kb0 ? a_ : ma; mb = pb >= kb0 ? b_ : mb; } else { ma = a_; mb = b_; } }
    SCHED;
  }
  float m = __builtin_fmaxf(ma, mb);
  { auto rr = __builtin_amdgcn_permlane32_swap(__float_as_uint(m), __float_as_uint(m), false, false);
    m = __builtin_fmaxf(__uint_as_float(rr[0]), __uint_as_float(rr[1])); }
  f32x16 cm;
#pragma unroll
  for (int r = 0; r < 16; ++r) mk0[r] = vsub(mk0[r], m);
  asm volatile("s_nop 1" : "+v"(mk0));
  QKA(0, sb[0], cm); SCHED;
#pragma unroll
  for (int r = 0; r < 16; ++r) cm[r] = -m;
  asm volatile("" : "+v"(cm));
  QKB(0, sb[0]); SCHED;
#pragma unroll
  for (int r = 0; r < 16; ++r) mk8[r] = vsub(mk8[r], m);
  vwait();
  f32x16 o = zero;
  f32x4 lsum = {0.f, 0.f, 0.f, 0.f};
  s16x4 vf[2][4]; unsigned pk[8]; float ex[2][2];
#define SB2(kb) sb[(kb) & 1]
#define VF(kb) do { _Pragma("unroll") for (int i_ = 0; i_ < 4; ++i_) vf[(kb) & 1][i_] = lds_tr(vbase + (kb) * 1024 + i_ * 256); } while (0)
#define PW(st) __builtin_bit_cast(bf16x8, (u32x4){pk[4 * (st)], pk[4 * (st) + 1], pk[4 * (st) + 2], pk[4 * (st) + 3]})
#define PVM(kb, st) do { if (!FIRST || (kb) >= kb0) o = __builtin_amdgcn_mfma_f32_32x32x16_bf16(cat8(vf[(kb) & 1][2 * (st)], vf[(kb) & 1][2 * (st) + 1]), PW(st), o, 0, 0, 0); } while (0)
#define SUM(kb, st) do { if (!FIRST || (kb) >= kb0) lsum = __builtin_amdgcn_mfma_f32_16x16x32_bf16(sel, PW(st), lsum, 0, 0, 0); } while (0)
  VF(0);
#pragma unroll
  for (int kb = 0; kb < 9; ++kb) {
#pragma unroll
    for (int j = 0; j < 8; ++j) {
      ex[j & 1][0] = __builtin_amdgcn_exp2f(SB2(kb)[2 * j]); ex[j & 1][1] = __builtin_amdgcn_exp2f(SB2(kb)[2 * j + 1]);
      asm volatile("" : "+v"(ex[j & 1][0]), "+v"(ex[j & 1][1])); SCHED;
      if (kb + j > 0) { pk[(j + 7) & 7] = cvtpk(ex[(j + 1) & 1][0], ex[(j + 1) & 1][1]); asm volatile("" : "+v"(pk[(j + 7) & 7])); }
      if (j == 0 && kb + 1 < 9) QKA(kb + 1, SB2(kb + 1), cm);
      if (j == 1 && kb > 0) PVM(kb - 1, 1);
      if (j == 2 && kb > 0) SUM(kb - 1, 1);
      if (j == 3 && kb + 1 < 9) QKB(kb + 1, SB2(kb + 1));
      if (j == 3 && kb + 1 < 9) VF(kb + 1);
      if (j == 5) PVM(kb, 0);
      if (j == 6) pf(kb);
      if (j == 7) SUM(kb, 0);
      SCHED;
    }
  }
  pk[7] = cvtpk(ex[1][0], ex[1][1]);
  PVM(8, 1); SUM(8, 1);
  const float l = (lane & 16) ? lsum[1] : lsum[0];
  float sc = gval * __builtin_amdgcn_rcpf(l);
  { float o0_ = o[0] + 0.0f; asm volatile("s_nop 7\n\ts_nop 7" : "+v"(sc), "+v"(o0_)); }
#pragma unroll
  for (int dh = 0; dh < 2; ++dh) {
    const unsigned a0 = cvtpk(vmul(o[8 * dh + 0], sc), vmul(o[8 * dh + 1], sc)), a1 = cvtpk(vmul(o[8 * dh + 2], sc), vmul(o[8 * dh + 3], sc));
    const unsigned b0 = cvtpk(vmul(o[8 * dh + 4], sc), vmul(o[8 * dh + 5], sc)), b1 = cvtpk(vmul(o[8 * dh + 6], sc), vmul(o[8 * dh + 7], sc));
    auto r0 = __builtin_amdgcn_permlane32_swap(a0, b0, false, false);
    auto r1 = __builtin_amdgcn_permlane32_swap(a1, b1, false, false);
    u32x4 w = {r0[0], r1[0], r0[1], r1[1]};
    st16_sc1(optr + (size_t)dh * S_ * 16, w);
  }
#undef QKA
#undef QKB
#undef VF
#undef PW
#undef PVM
#undef SUM
#undef SB2
}

__device__ __forceinline__ void wait_count(unsigned* c, unsigned want) {
  unsigned spins = 0;
  while ((unsigned)__builtin_amdgcn_readfirstlane(__hip_atomic_load(c, __ATOMIC_RELAXED, __HIP_MEMORY_SCOPE_AGENT)) < want) {
    __builtin_amdgcn_s_sleep(4);
    if (++spins > (1u << 21)) break;
  }
  asm volatile("" ::: "memory");
}

__global__ __launch_bounds__(512, 2) void k2_fused(const u16* __restrict__ wd, const u16* __restrict__ wo, char* __restrict__ ws,
                                                   u16* __restrict__ out) {
  extern __shared__ __attribute__((aligned(16))) char lds_[];
  char LAS* lds = (char LAS*)lds_;
  const unsigned lds0 = (unsigned)(uintptr_t)lds;
  const int tid = threadIdx.x, lane = tid & 63;
  const int wid = __builtin_amdgcn_readfirstlane(tid >> 6);
  const int bid = blockIdx.x;
  const int xcd = bid & 7, idx = bid >> 3;
  const int sp = xcd >> 1, tn = idx & 7, tm = (xcd & 1) * 4 + (idx >> 3);
  const int mp = sp >> 1, np = sp & 1;
  const int tile = tm * 8 + tn;
  const int head = sp * 8 + tn, rb = tm;
  const int q0 = rb * 256;
  const char* qkv = ws + WS_QKV;
  unsigned* cntA = (unsigned*)(ws + WS_CNT);
  unsigned* cntP = (unsigned*)(ws + WS_CNT + 16 * 128);
  STAMP_DECL;
  STAMP(1, 0);

  const int qrow = q0 + wid * 32 + (lane & 31);
  const int rowlo = (rb > 0 ? q0 - 256 : 0) + wid * 32, rowhi = q0 + wid * 32;
  bf16x8 qf[2];
  float gval;
  {
    const char* qp = qkv + ((size_t)(head * 2) * S_ + q0 + wid * 32) * 32;
    const unsigned voffQ = (lane & 31) * 32 + (lane >> 5) * 16;
    qf[0] = *(const bf16x8*)(qp + voffQ);
    qf[1] = *(const bf16x8*)(qp + S_ * 32 + voffQ);
    gval = ((const float*)(ws + WS_G))[qrow];
    const unsigned voffK = (lane >> 1) * 32 + (((lane & 1) ^ ((lane >> 4) & 1)) << 4);

#pragma unroll
    for (int dh = 0; dh < 2; ++dh) {
      glds16(qkv + ((size_t)(64 + head * 2 + dh) * S_ + rowhi) * 32, voffK, lds0 + AT_K + dh * 16384 + (wid + 8) * 1024);
      glds16(qkv + ((size_t)(64 + head * 2 + dh) * S_ + rowlo) * 32, voffK, lds0 + AT_K + dh * 16384 + wid * 1024);
    }
  }

  const int ld = wid & 3;
  unsigned voffA, voffB0, voffB1m;
  auto set_voff = [&]() {
    const int l_ = fresh_lane();
    const int r = l_ >> 3, j = l_ & 7, q = r & 3;
    const int f0 = (q >> 1) & 1, f1 = f0 | 2;
    voffA = l_ * 16;
    voffB0 = r * 2048 + ((((j >> 1) ^ f0) << 1 | (j & 1)) << 4);
    voffB1m = (r + 8) * 2048 + ((((j >> 1) ^ f1) << 1 | (j & 1)) << 4) - 1024;
  };
  const char* hA = ws + WS_A2 + ((size_t)(sp * NH * 2 + (ld >> 1)) * S_ + tm * 256 + ((ld & 1) ^ mp) * 128) * 32;
  const char* hB = (const char*)wd + (size_t)(sp * NH * 32 + 16 * (ld & 1)) * 2048 + (size_t)(tn * 128 + ((ld >> 1) ^ np) * 64) * 2;
  const char* oA = ws + WS_A2 + ((size_t)(224 + sp * NO * 2 + (ld >> 1)) * S_ + tm * 256 + ((ld & 1) ^ mp) * 128) * 32;
  const char* oB = (const char*)wo + (size_t)(sp * NO * 32 + 16 * (ld & 1)) * 2048 + (size_t)(tn * 128 + ((ld >> 1) ^ np) * 64) * 2;
  const char* cA = ws + WS_A2 + ((size_t)(224 + 8 * NO + ld) * S_ + tm * 256 + mp * 128) * 32;
  const char* cB = (const char*)wo + (size_t)(128 * NO + 16 * ld) * 2048 + (size_t)(tn * 128 + np * 64) * 2;
  auto issue = [&](int i) {
    const unsigned dst = lds0 + (i % 6) * K2_SLOT;
    if (i < NH) {
      glds16x4<0>(hA + (size_t)i * (2 * S_ * 32), voffA, dst + ld * 4096);
      glds16x2(hB + (size_t)i * (32 * 2048), voffB0, voffB1m, dst + 16384 + ld * 2048);
    } else if (i < NK) {
      glds16x4<1>(oA + (size_t)(i - NH) * (2 * S_ * 32), voffA, dst + ld * 4096);
      glds16x2(oB + (size_t)(i - NH) * (32 * 2048), voffB0, voffB1m, dst + 16384 + ld * 2048);
    } else {
      glds16x4<1>(cA + (size_t)(i - NK) * (4 * S_ * 32), voffA, dst + ld * 4096);
      glds16x2(cB + (size_t)(i - NK) * (64 * 2048), voffB0, voffB1m, dst + 16384 + ld * 2048);
    }
  };

  {
    set_voff();
    const int hf = wid >> 2;
    unsigned voffBp = hf ? voffB1m + 1024 : voffB0;
    const char* pA = hA + hf * 2048;
    const unsigned dA = lds0 + ld * 4096 + hf * 2048, dB = lds0 + 16384 + ld * 2048 + hf * 1024;
    auto kwait = [&]() {
      asm volatile("" : "+v"(voffA), "+v"(voffBp));
      asm volatile("s_waitcnt vmcnt(0)" : "+v"(qf[0]), "+v"(qf[1]), "+v"(gval) :: "memory");
      BAR;
      asm volatile("" ::: "memory");
      STAMP(1, 1);
    };
    auto vload = [&]() {
#pragma unroll
      for (int dh = 0; dh < 2; ++dh) {
        glds16(qkv + ((size_t)(128 + head * 2 + dh) * S_ + rowlo) * 32, voffA, lds0 + (dh ? AT_V1 : AT_V) + wid * 1024);
        glds16(qkv + ((size_t)(128 + head * 2 + dh) * S_ + rowhi) * 32, voffA, lds0 + (dh ? AT_V1 : AT_V) + (wid + 8) * 1024);
      }
      if (lane < 32) *(LAS float*)(lds + K2_G + (wid * 32 + lane) * 4) = 1.0f - gval;
    };
    auto vwait = [&]() { WAIT_V(0); BAR; asm volatile("" ::: "memory"); };
    auto pf = [&](int kb) {
      const int st = kb / 3, j = kb % 3;
      if (j < 2) glds16(pA + (size_t)st * (2 * S_ * 32) + j * 1024, voffA, dA + st * K2_SLOT + j * 1024);
      else glds16(hB + (size_t)st * (32 * 2048), voffBp, dB + st * K2_SLOT);
    };
    if (rb == 0) attn_wave<true>(lds, ws, wid, lane, head, qrow, qf, gval, kwait, vload, vwait, pf);
    else attn_wave<false>(lds, ws, wid, lane, head, qrow, qf, gval, kwait, vload, vwait, pf);
  }
  STAMP(1, 2);

  if (wid >= 4) {
    set_voff();
    WAIT_V(8);
    WAIT_L0; BAR;
    issue(3); issue(4);
    WAIT_V(14);
    BAR;
    unsigned* ca = cntA + tm * 32;
    unsigned fl = 0;
    for (int P = 0; P < NS / 2; ++P) {
      const int a = 2 * P + 5;
      if (P == 9) asm volatile("global_load_dword %0, %1, off sc1" : "=v"(fl) : "v"(ca) : "memory");
      if (P == 11) {
        asm volatile("" : "+v"(fl));
        if ((unsigned)__builtin_amdgcn_readfirstlane(fl) < 256u) wait_count(ca, 256u);
      }
      if (a + 1 < NS) { issue(a); issue(a + 1); WAIT_V(12); }
      else if (a < NS) { issue(a); WAIT_V(6); }
      else { WAIT_V(0); }
      if (P == 0 && voffA == 0) __hip_atomic_fetch_add(ca, 1u, __ATOMIC_RELAXED, __HIP_MEMORY_SCOPE_AGENT);
      if (P + 1 < NS / 2) BAR;
    }
    STAMP(1, 3); STAMP(1, 4); STAMP(1, 5); STAMP(1, 6);
    STAMP_END(1, 7);
    return;
  }

  const int wm = wid & 1, wn = wid >> 1;
  const int fr = lane & 15, g4 = lane >> 4;
  const unsigned rAh = lds0 + (g4 >> 1) * 8192 + (wm * 64 + fr) * 32 + (g4 & 1) * 16;
  unsigned rAc = 0;
  unsigned rB0, rB1;
  { const int q = (lane & 15) >> 2, p = lane & 3;
    const int f = ((q >> 1) & 1) | ((g4 & 1) << 1);
    rB0 = lds0 + 16384 + g4 * 1024 + q * 128 + (((wn * 2) ^ f) << 5) + p * 8;
    rB1 = rB0 ^ 32u; }

  f32x4 acc[8][4];
#pragma unroll
  for (int m = 0; m < 8; ++m)
#pragma unroll
    for (int n = 0; n < 4; ++n) acc[m][n] = (f32x4){0.f, 0.f, 0.f, 0.f};
  bf16x8 A0[8], A1[8];
  s16x4 L0[4], H0[4], L1[4], H1[4];
#define RD(AF, BL, BH, k, so, COV) do { \
    if ((k) < 8) { const int n_ = (k) >> 1; const unsigned b_ = ((n_ & 1) ? rB1 : rB0) + (so); \
      if ((k) & 1) BH[n_] = lds_tr(LP(b_) + (n_ >> 1) * 4096 + 512); else BL[n_] = lds_tr(LP(b_) + (n_ >> 1) * 4096); } \
    else { const int m_ = (k) - 8; \
      if (COV) AF[m_] = lds_b128(LP(rAc + (so)) + (m_ >> 2) * 8192 + (m_ & 3) * 512); \
      else AF[m_] = lds_b128(LP(rAh + (so)) + (m_ >> 2) * 4096 + (m_ & 3) * 512); } } while (0)
#define MFH(AF, BL, BH, k) acc[(k) >> 2][(k) & 3] = __builtin_amdgcn_mfma_f32_16x16x32_bf16(cat8(BL[(k) & 3], BH[(k) & 3]), AF[(k) >> 2], acc[(k) >> 2][(k) & 3], 0, 0, 0)
#define STAGE_H(AC, LC, HC, AN, LN, HN, so, COV) do { \
    _Pragma("unroll") for (int k = 0; k < 32; ++k) { MFH(AC, LC, HC, k); if (k >= 1 && k < 17) RD(AN, LN, HN, k - 1, so, COV); SCHED; } } while (0)

  WAIT_V(8); WAIT_L0; BAR;
  asm volatile("" ::: "memory");
#pragma unroll
  for (int k = 0; k < 16; ++k) RD(A0, L0, H0, k, 0, 0);
  WAIT_V(2); WAIT_L0; BAR;
  SCHED;
  STAMP(1, 3);
  int s1 = 1;
#pragma unroll 1
  for (int ph = 0; ph < 2; ++ph) {
    const int np_ = ph ? (NO > 0 ? NO / 2 - 1 : 0) : (NO > 0 ? NH / 2 : NH / 2 - 1);
#pragma unroll 1
    for (int P = 0; P < np_; ++P) {
      const int s2 = (s1 == 5) ? 0 : s1 + 1;
      STAGE_H(A0, L0, H0, A1, L1, H1, s1 * K2_SLOT, 0);
      STAGE_H(A1, L1, H1, A0, L0, H0, s2 * K2_SLOT, 0);
      s1 = s2 + 1;
      if (P == 2) { WAIT_V(0); if (fresh_lane() == 0) __hip_atomic_fetch_add(cntA + tm * 32, 1u, __ATOMIC_RELAXED, __HIP_MEMORY_SCOPE_AGENT); }
      WAIT_L0; BAR; SCHED;
    }
    if (ph == 0 && NO > 0) {
      const int l4 = fresh_lane();
#pragma unroll
      for (int m = 0; m < 8; ++m) {
        const float sc = *(const LAS float*)(lds + K2_G + (((m >> 2) ^ mp) * 128 + wm * 64 + (m & 3) * 16 + (l4 & 15)) * 4);
#pragma unroll
        for (int n = 0; n < 4; ++n) acc[m][n] *= sc;
      }
      SCHED;
    }
  }
  STAGE_H(A0, L0, H0, A1, L1, H1, ((NK - 1) % 6) * K2_SLOT, 0);
  { const int l2 = fresh_lane(); rAc = lds0 + (l2 >> 5) * 4096 + (wm * 64 + (l2 & 15)) * 32 + ((l2 >> 4) & 1) * 16; }
  STAGE_H(A1, L1, H1, A0, L0, H0, (NK % 6) * K2_SLOT, 1);
  WAIT_L0; BAR; SCHED;
  STAMP(1, 4);

  auto rs = __builtin_amdgcn_make_buffer_rsrc((void*)(ws + WS_PART), 0, 16u << 20, 0x00020000);
  const int l3 = fresh_lane();
  if (NO == 0) {
#pragma unroll
    for (int m = 0; m < 8; ++m) {
      const float sc = *(const LAS float*)(lds + K2_G + (((m >> 2) ^ mp) * 128 + wm * 64 + (m & 3) * 16 + (l3 & 15)) * 4);
#pragma unroll
      for (int n = 0; n < 4; ++n) acc[m][n] *= sc;
    }
  }
#pragma unroll
  for (int d = 3; d >= 1; --d) {
    const int h = d >> 1, c = d & 1;
#pragma unroll
    for (int mb = 0; mb < 4; ++mb) {
      const f32x4 u = acc[4 * h + mb][2 * c], v = acc[4 * h + mb][2 * c + 1];
      u32x4 w = {cvtpk(u[0], u[1]), cvtpk(u[2], u[3]), cvtpk(v[0], v[1]), cvtpk(v[2], v[3])};
      const unsigned off = (((((unsigned)tile * 4 + (d ^ sp)) * 4 + sp) * 4 + wid) * 4 + mb) * 1024u + l3 * 16;
      __builtin_amdgcn_raw_buffer_store_b128(w, rs, off, 0, 16);
    }
  }
  f32x4 acK[4][2];
#pragma unroll
  for (int mb = 0; mb < 4; ++mb)
#pragma unroll
    for (int j = 0; j < 2; ++j) acK[mb][j] = acc[mb][j];
  SCHED;
#define MFC(AF, BL, BH, k) acK[((k) >> 1) & 3][(k) & 1] = __builtin_amdgcn_mfma_f32_16x16x32_bf16(cat8(BL[2 * ((k) >> 3) + ((k) & 1)], BH[2 * ((k) >> 3) + ((k) & 1)]), \
                                                             AF[4 * ((k) >> 3) + (((k) >> 1) & 3)], acK[((k) >> 1) & 3][(k) & 1], 0, 0, 0)
#define STAGE_C(AC, LC, HC, AN, LN, HN, so) do { \
    _Pragma("unroll") for (int k = 0; k < 16; ++k) { MFC(AC, LC, HC, k); RD(AN, LN, HN, k, so, 1); SCHED; } } while (0)
  u32x4 part[3][4];
  unsigned* myc = cntP + (tile * 4 + wid) * 32;
  unsigned flagv = 0;
#define XSIG 2
#define XLD 2
#pragma unroll
  for (int P = NK / 2; P < NS / 2; ++P) {
    STAGE_C(A0, L0, H0, A1, L1, H1, ((2 * P + 1) % 6) * K2_SLOT);
    if (P + 1 < NS / 2) { STAGE_C(A1, L1, H1, A0, L0, H0, ((2 * P + 2) % 6) * K2_SLOT); }
    else {
#pragma unroll
      for (int k = 0; k < 16; ++k) MFC(A1, L1, H1, k);
    }
    if (P == NK / 2 + XSIG) { WAIT_V(0); if (l3 == 0) __hip_atomic_fetch_add(myc, 1u, __ATOMIC_RELAXED, __HIP_MEMORY_SCOPE_AGENT); }
    if (P == NS / 2 - 2 - XLD) flagv = __hip_atomic_load(myc, __ATOMIC_RELAXED, __HIP_MEMORY_SCOPE_AGENT);
    if (P + 1 < NS / 2) { WAIT_L0; BAR; SCHED; }
    if (P == NS / 2 - 1 - XLD) {
      if ((unsigned)__builtin_amdgcn_readfirstlane(flagv) < 4u) wait_count(myc, 4u);
      asm volatile("" ::: "memory");
#pragma unroll
      for (int jj = 0; jj < 3; ++jj) {
        const int src = (sp + 1 + jj) & 3;
#pragma unroll
        for (int mb = 0; mb < 4; ++mb) {
          const unsigned off = (((((unsigned)tile * 4 + sp) * 4 + src) * 4 + wid) * 4 + mb) * 1024u + l3 * 16;
          part[jj][mb] = __builtin_amdgcn_raw_buffer_load_b128(rs, off, 0, 16);
        }
      }
    }
  }
  STAMP(1, 5);
  const int lane_e = fresh_lane();
#pragma unroll
  for (int mb = 0; mb < 4; ++mb) {
    f32x4 u = acK[mb][0], v = acK[mb][1];
#pragma unroll
    for (int jj = 0; jj < 3; ++jj) {
      const u32x4 w = part[jj][mb];
      u[0] += __uint_as_float(w[0] << 16); u[1] += __uint_as_float(w[0] & 0xffff0000u);
      u[2] += __uint_as_float(w[1] << 16); u[3] += __uint_as_float(w[1] & 0xffff0000u);
      v[0] += __uint_as_float(w[2] << 16); v[1] += __uint_as_float(w[2] & 0xffff0000u);
      v[2] += __uint_as_float(w[3] << 16); v[3] += __uint_as_float(w[3] & 0xffff0000u);
    }
    auto r0 = __builtin_amdgcn_permlane16_swap(cvtpk(u[0], u[1]), cvtpk(v[0], v[1]), false, false);
    auto r1 = __builtin_amdgcn_permlane16_swap(cvtpk(u[2], u[3]), cvtpk(v[2], v[3]), false, false);
    u32x4 w = {r0[0], r1[0], r0[1], r1[1]};
    const int row = tm * 256 + mp * 128 + wm * 64 + mb * 16 + (lane_e & 15);
    const int col = tn * 128 + np * 64 + wn * 32 + ((lane_e >> 4) & 1) * 16 + (lane_e >> 5) * 8;
    *(u32x4*)(out + (size_t)row * D_ + col) = w;
  }
  STAMP(1, 6);
  STAMP_END(1, 7);
}

constexpr int G1_LDS_A = 0;
constexpr int G1_LDS_B = 65536;
constexpr int G1_LDS_B4 = 131072;
constexpr int G1_LDS_GW = 147456;
constexpr int G1_LDS_DUMMY = G1_LDS_GW + 2048;
constexpr int G1_LDS = G1_LDS_DUMMY + 2048;

#define QMODE 2
#define HMODE 2
#define TBACK 0
template <int MODE, int OFF>
__device__ __forceinline__ void store_pair16(const void* sbase, unsigned voff, unsigned x0, unsigned x1, unsigned y0, unsigned y1) {
  auto r0 = __builtin_amdgcn_permlane16_swap(x0, y0, false, false);
  auto r1 = __builtin_amdgcn_permlane16_swap(x1, y1, false, false);
  u32x4 w = {r0[0], r1[0], r0[1], r1[1]};
  if (MODE == 0) asm volatile("global_store_dwordx4 %0, %1, %2 offset:%3\n\ts_nop 1" ::"v"(voff), "v"(w), "s"(sbase), "n"(OFF) : "memory");
  else if (MODE == 1) asm volatile("global_store_dwordx4 %0, %1, %2 offset:%3 nt\n\ts_nop 1" ::"v"(voff), "v"(w), "s"(sbase), "n"(OFF) : "memory");
  else asm volatile("global_store_dwordx4 %0, %1, %2 offset:%3 sc1\n\ts_nop 1" ::"v"(voff), "v"(w), "s"(sbase), "n"(OFF) : "memory");
}

__global__ __launch_bounds__(512, 2) void k_gemm1(const u16* __restrict__ x, const u16* __restrict__ wqkv, const u16* __restrict__ wg,
                                                  const u16* __restrict__ wu, const u16* __restrict__ gate_w, char* __restrict__ ws,
                                                  const u16* __restrict__ wd, const u16* __restrict__ wo, const u16* outp) {
  extern __shared__ __attribute__((aligned(16))) char lds_[];
  char LAS* lds = (char LAS*)lds_;
  const unsigned lds0 = (unsigned)(uintptr_t)lds;
  const int tid = threadIdx.x, lane = tid & 63;
  const int wid = __builtin_amdgcn_readfirstlane(tid >> 6);
  const int wr = wid >> 2, wc = wid & 3;
  const int bid = blockIdx.x;
  const int xcd = bid & 7, idx = bid >> 3;
  const int sp = xcd >> 1, tm = (xcd & 1) * 4 + (idx >> 3), ct = idx & 7;
  const int tn = sp * 8 + ct;

  int ptype[5], pcol[5];
  {
    auto qc = [&](int i) { return (i >> 2) * 1024 + 256 * sp + 64 * (i & 3); };
    if (ct < 6) {
      ptype[0] = 1; ptype[1] = 2; pcol[0] = pcol[1] = 896 * sp + 128 * ct;
      ptype[2] = 1; ptype[3] = 2; pcol[2] = pcol[3] = 896 * sp + 128 * ct + 64;
      ptype[4] = 0; pcol[4] = qc(ct);
    } else {
      ptype[0] = 1; ptype[1] = 2; pcol[0] = pcol[1] = 896 * sp + 768 + 64 * (ct - 6);
      ptype[2] = ptype[3] = ptype[4] = 0;
      pcol[2] = qc(6 + 3 * (ct - 6)); pcol[3] = qc(7 + 3 * (ct - 6)); pcol[4] = qc(8 + 3 * (ct - 6));
    }
  }

  const char* sA = (const char*)x + (size_t)(tm * 256 + wid * 8) * 2048;
  unsigned voffA;
  { const int r = lane >> 3, j = lane & 7; voffA = r * 2048 + ((j ^ ((4 * (wid & 1) + (r >> 1)) & 7)) << 4); }
  const unsigned ldsA = lds0 + G1_LDS_A + wid * 1024;
  const int myp = wid >> 1;
  int mt = ptype[0], mc = pcol[0];
#pragma unroll
  for (int p = 1; p < 4; ++p) if (myp == p) { mt = ptype[p]; mc = pcol[p]; }
  const unsigned strB = (mt == 0) ? 6144u : 7168u;
  const char* sB = (const char*)(mt == 0 ? wqkv : (mt == 1 ? wg : wu)) + (size_t)mc * 2 + (size_t)(16 * (wid & 1)) * strB;
  unsigned voffB0, voffB1;
  { const int r = lane >> 3, j = lane & 7, q = r & 3;
    const int f0 = (q >> 1) & 1, f1 = f0 | 2;
    voffB0 = r * strB + ((((j >> 1) ^ f0) << 1 | (j & 1)) << 4);
    voffB1 = (r + 8) * strB + ((((j >> 1) ^ f1) << 1 | (j & 1)) << 4); }
  const unsigned ldsB = lds0 + G1_LDS_B + myp * 8192 + (wid & 1) * 2048;
  constexpr unsigned strB4 = 6144u;
  const char* sB4 = (const char*)wqkv + (size_t)pcol[4] * 2 + (size_t)(8 * wid) * strB4;
  unsigned voffB4;
  { const int r = lane >> 3, j = lane & 7, q = r & 3;
    const int f = ((q >> 1) & 1) | ((wid & 1) << 1);
    voffB4 = r * strB4 + ((((j >> 1) ^ f) << 1 | (j & 1)) << 4); }
  const unsigned ldsB4 = lds0 + G1_LDS_B4 + wid * 1024;

#define KC(kt) ((unsigned)((kt) < 15 ? (kt) : 15))
#define G1_A0(kt, b) do { const char* _p = sA + KC(kt) * 128u; glds16(_p, voffA, ldsA + (b) * 32768); \
                          glds16(_p + 64 * 2048, voffA, ldsA + (b) * 32768 + 8192); } while (0)
#define G1_A1(kt, b) do { const char* _p = sA + KC(kt) * 128u; glds16(_p + 128 * 2048, voffA, ldsA + (b) * 32768 + 16384); \
                          glds16(_p + 192 * 2048, voffA, ldsA + (b) * 32768 + 24576); } while (0)
#define G1_X(kt, b) do { const char* _p = sB + KC(kt) * 64u * strB; glds16(_p, voffB0, ldsB + (b) * 32768); \
                         glds16(_p, voffB1, ldsB + (b) * 32768 + 1024); } while (0)
#define G1_Y(kt, b) do { const char* _p = sB + (KC(kt) * 64u + 32u) * strB; glds16(_p, voffB0, ldsB + (b) * 32768 + 4096); \
                         glds16(_p, voffB1, ldsB + (b) * 32768 + 4096 + 1024); } while (0)
#define G1_Z(kt, b) glds16(sB4 + KC(kt) * 64u * strB4, voffB4, ldsB4 + (b) * 8192)

  STAMP_DECL;
  STAMP(0, 0);
  glds4((const char*)gate_w + wid * 256, lane * 4, lds0 + G1_LDS_GW + wid * 256);
  G1_X(0, 0); G1_A0(0, 0); G1_A1(0, 0); G1_Z(0, 0); G1_Y(0, 0); G1_X(1, 1);
#define TOUCH 1
#define TD 5
#define TPRO 3
#define CODEPF 1
  const char* tp; unsigned voffT, tstep, tlead, tmax = 15;
  if (wid == 0) { tp = (const char*)x + (size_t)(tm * 256 + ct * 32) * 2048; voffT = (lane & 31) * 2048; tstep = 128; tlead = TD; }
  else if (wid <= 5) {
    int t_ = ptype[0], c_ = pcol[0];
#pragma unroll
    for (int p = 1; p < 5; ++p) if (wid - 1 == p) { t_ = ptype[p]; c_ = pcol[p]; }
    const unsigned st_ = (t_ == 0) ? 6144u : 7168u;
    tp = (const char*)(t_ == 0 ? wqkv : (t_ == 1 ? wg : wu)) + (size_t)c_ * 2 + (size_t)((tm & 3) * 16) * st_;
    voffT = (lane & 15) * st_; tstep = 64 * st_; tlead = TD;
  } else if (wid == 6) { tp = (const char*)wd + (size_t)bid * 28672; voffT = (lane % 14) * 128; tstep = 1792; tlead = 1; }
  else if (idx >= 16 || !CODEPF) { tp = (const char*)wo + (size_t)(xcd * 16 + (idx & 15)) * 16384; voffT = (lane & 7) * 128; tstep = 1024; tlead = 1; }
  else {
    unsigned long long pc; int d;
    asm volatile("s_getpc_b64 %0\n\ts_mov_b32 %1, _Z8k2_fusedPKtS0_PcPt@rel32@lo+4" : "=s"(pc), "=s"(d));
    const bool ok = d < -4096;
    const unsigned n4k = ok ? (unsigned)(-d) / 4096u : 1u;
    tp = ok ? (const char*)(pc + (long long)d) : (const char*)wo;
    voffT = (lane & 31) * 128; tstep = 4096; tlead = (unsigned)-8; tmax = (n4k < 7u ? n4k : 7u) - 1u;
  }
  auto touch = [&](int j) { const unsigned k_ = (unsigned)j + tlead; glds4(tp + (k_ < tmax ? k_ : tmax) * tstep, voffT, lds0 + G1_LDS_DUMMY + wid * 256); };

  const int fr = lane & 15, g4 = lane >> 4;
  const char LAS* rA0 = lds + G1_LDS_A + wr * 8192 + fr * 128 + (((0 + g4) ^ (fr >> 1)) << 4);
  const char LAS* rA1 = lds + G1_LDS_A + wr * 8192 + fr * 128 + (((4 + g4) ^ (fr >> 1)) << 4);
  const char LAS* rB; const char LAS* rB4;
  { const int q = (lane & 15) >> 2, p = lane & 3;
    const int f = ((q >> 1) & 1) | ((g4 & 1) << 1);
    const int o = g4 * 1024 + q * 128 + ((wc ^ f) << 5) + p * 8;
    rB = lds + G1_LDS_B + o;
    rB4 = lds + G1_LDS_B4 + o; }

  const char LAS* rG; const char LAS* rGW;
  { const int r = tn * 8 + wid, l = lane & 31;
    rG = lds + G1_LDS_A + r * 128 + (((l >> 2) ^ ((r >> 1) & 7)) << 4) + (l & 3) * 4;
    rGW = lds + G1_LDS_GW + l * 4; }
  float gsum = 0.f;
  f32x4 acc[2][4][5];
#pragma unroll
  for (int a = 0; a < 2; ++a)
#pragma unroll
    for (int m = 0; m < 4; ++m)
#pragma unroll
      for (int p = 0; p < 5; ++p) acc[a][m][p] = (f32x4){0.f, 0.f, 0.f, 0.f};
  bf16x8 Af[8], Bf[5];

#define G1_LDB(b, ks) do { _Pragma("unroll") for (int p = 0; p < 4; ++p) { \
      s16x4 lo = lds_tr(rB + (b) * 32768 + p * 8192 + (ks) * 4096), hi = lds_tr(rB + (b) * 32768 + p * 8192 + (ks) * 4096 + 512); Bf[p] = cat8(lo, hi); } \
    { s16x4 lo = lds_tr(rB4 + (b) * 8192 + (ks) * 4096), hi = lds_tr(rB4 + (b) * 8192 + (ks) * 4096 + 512); Bf[4] = cat8(lo, hi); } } while (0)
#define G1_LDA(b, ks) do { _Pragma("unroll") for (int m = 0; m < 8; ++m) \
      Af[m] = lds_b128(((ks) ? rA1 : rA0) + (b) * 32768 + (m >> 2) * 16384 + (m & 3) * 2048); } while (0)
#define EARLYB 0
#define G1_MMA() do { __builtin_amdgcn_s_setprio(1); _Pragma("unroll") for (int k = 0; k < 40; ++k) { const int m = k / 5, p = k % 5; \
      if (k == 40 - EARLYB) { SCHED; __builtin_amdgcn_s_setprio(3); BAR; SCHED; } \
      acc[m >> 2][m & 3][p] = __builtin_amdgcn_mfma_f32_16x16x32_bf16(Bf[p], Af[m], acc[m >> 2][m & 3][p], 0, 0, 0); } \
      __builtin_amdgcn_s_setprio(0); if (EARLYB == 0) BAR; } while (0)
#define WV_A WAIT_V(5)
#define WV_B WAIT_V(10)
  WAIT_V(4); BAR;
  STAMP(0, 1);
  if (TOUCH) {
    if (wid < 6) {
#pragma unroll
      for (int e = TPRO; e >= 2; --e) touch(-e);
    } else {
      const char* b0 = wid == 6 ? (const char*)ws : (const char*)outp;
      const char* b1 = wid == 6 ? (const char*)wo : (const char*)wd;
      const unsigned n0 = wid == 6 ? 46u : 4u, n1 = wid == 6 ? 2u : 7u;
      glds4(b0, ((unsigned)lane % n0) << 20, lds0 + G1_LDS_DUMMY + wid * 256);
      glds4(b1, ((unsigned)lane % n1) << 20, lds0 + G1_LDS_DUMMY + wid * 256);
    }
    touch(-1);
  }
  if (wr == 1) BAR;

#define G1_TILE(t, b) do { \
    G1_LDB(b, 0); G1_LDA(b, 0); \
    G1_A0((t) + 1, (b) ^ 1); G1_A1((t) + 1, (b) ^ 1); G1_Z((t) + 1, (b) ^ 1); G1_Y((t) + 1, (b) ^ 1); \
    WV_B; WAIT_L0; BAR; G1_MMA(); SCHED; \
    G1_LDB(b, 1); G1_LDA(b, 1); \
    { const unsigned xa = *(const LAS unsigned*)(rG + (b) * 32768), wa = *(const LAS unsigned*)(rGW + (t) * 128); \
      G1_X((t) + 2, b); if (TOUCH) touch(t); \
      WAIT_L0; \
      gsum = __builtin_fmaf(__uint_as_float(xa << 16), __uint_as_float(wa << 16), gsum); \
      gsum = __builtin_fmaf(__uint_as_float(xa & 0xffff0000u), __uint_as_float(wa & 0xffff0000u), gsum); \
      asm volatile("" : "+v"(gsum)); } \
    WV_A; BAR; G1_MMA(); SCHED; \
  } while (0)

#pragma unroll 1
  for (int t = 0; t < 16; t += 2) {
    G1_TILE(t, 0);
    G1_TILE(t + 1, 1);
  }
  STAMP(0, 2);
  const int lane_e = fresh_lane();
#define lane lane_e

  {
#pragma unroll
    for (int o = 16; o >= 1; o >>= 1) gsum += __shfl_xor(gsum, o);
    if (lane == 0) ((float*)(ws + WS_G))[tm * 256 + tn * 8 + wid] = 1.0f / (1.0f + __expf(-gsum));
    if (wid == 0 && lane == 0) ((unsigned*)(ws + WS_CNT))[(16 + bid) * 32] = 0u;
    if (wid == 1 && lane == 0 && bid < 16) ((unsigned*)(ws + WS_CNT))[bid * 32] = 0u;
  }

  const unsigned voffS = (((lane >> 4) & 1) * 16 + (lane & 15)) * 32 + (lane >> 5) * 16;
  const size_t rowoff = (size_t)(tm * 256 + wr * 64) * 32 + 2048;
#define G1_STQ(p) do { const float sc = pcol[p] < D_ ? QSCALE : 1.0f; \
    const char* dst = ws + WS_QKV + (size_t)(pcol[p] / 16 + wc) * (S_ * 32) + rowoff; \
    { const f32x4 u = acc[0][0][p] * sc, v = acc[0][1][p] * sc; store_pair16<QMODE, -2048>(dst, voffS, cvtpk(u[0], u[1]), cvtpk(u[2], u[3]), cvtpk(v[0], v[1]), cvtpk(v[2], v[3])); } \
    { const f32x4 u = acc[0][2][p] * sc, v = acc[0][3][p] * sc; store_pair16<QMODE, -1024>(dst, voffS, cvtpk(u[0], u[1]), cvtpk(u[2], u[3]), cvtpk(v[0], v[1]), cvtpk(v[2], v[3])); } \
    { const f32x4 u = acc[1][0][p] * sc, v = acc[1][1][p] * sc; store_pair16<QMODE, 2048>(dst, voffS, cvtpk(u[0], u[1]), cvtpk(u[2], u[3]), cvtpk(v[0], v[1]), cvtpk(v[2], v[3])); } \
    { const f32x4 u = acc[1][2][p] * sc, v = acc[1][3][p] * sc; store_pair16<QMODE, 3072>(dst, voffS, cvtpk(u[0], u[1]), cvtpk(u[2], u[3]), cvtpk(v[0], v[1]), cvtpk(v[2], v[3])); } } while (0)
#define G1_H1(pp, a, m, OFF) do { const f32x4 g0 = acc[a][m][2 * (pp)], u0 = acc[a][m][2 * (pp) + 1], g1 = acc[a][(m) + 1][2 * (pp)], u1 = acc[a][(m) + 1][2 * (pp) + 1]; \
    const f32x2 h0 = geglu2(g0.xy, u0.xy), h1 = geglu2(g0.zw, u0.zw), h2 = geglu2(g1.xy, u1.xy), h3 = geglu2(g1.zw, u1.zw); \
    store_pair16<HMODE, OFF>(dst, voffS, cvtpk(h0.x, h0.y), cvtpk(h1.x, h1.y), cvtpk(h2.x, h2.y), cvtpk(h3.x, h3.y)); } while (0)
#define G1_STH(pp) do { const char* dst = ws + WS_A2 + (size_t)(pcol[2 * (pp)] / 16 + wc) * (S_ * 32) + rowoff; \
    G1_H1(pp, 0, 0, -2048); G1_H1(pp, 0, 2, -1024); G1_H1(pp, 1, 0, 2048); G1_H1(pp, 1, 2, 3072); } while (0)
#define G1_TB(p) glds4(ws + WS_QKV + (size_t)(pcol[p] / 16 + wc) * (S_ * 32) + rowoff - 2048, voffTB, lds0 + G1_LDS_DUMMY + wid * 256)
  const unsigned voffTB = ((lane >> 4) & 1) * 4096 + (lane & 15) * 128;
  G1_STQ(4);
  if (wr == 0) BAR;
  WAIT_V(4);
  if (ptype[2] == 0) {
    G1_STQ(2); G1_STQ(3);
    G1_STH(0);
    if (TBACK) { WAIT_V(4); G1_TB(4); G1_TB(2); G1_TB(3); }
  } else {
    G1_STH(0);
    if (TBACK) { WAIT_V(4); G1_TB(4); }
    G1_STH(1);
  }
#undef lane
  STAMP(0, 3);
  STAMP(0, 4);
  STAMP_END(0, 5);
}

extern "C" void kernel_launch(void* const* d_in, const int* in_sizes, int n_in, void* d_out, int out_size, void* d_ws, size_t ws_size,
                              hipStream_t stream) {
  const u16* x = (const u16*)d_in[0];
  const u16* wqkv = (const u16*)d_in[1];
  const u16* wo = (const u16*)d_in[2];
  const u16* wg = (const u16*)d_in[4];
  const u16* wu = (const u16*)d_in[5];
  const u16* wd = (const u16*)d_in[6];
  const u16* gate_w = (const u16*)d_in[7];
  char* ws = (char*)d_ws;
  (void)hipFuncSetAttribute((const void*)k_gemm1, hipFuncAttributeMaxDynamicSharedMemorySize, G1_LDS);
  (void)hipFuncSetAttribute((const void*)k2_fused, hipFuncAttributeMaxDynamicSharedMemorySize, K2_LDS);
  k_gemm1<<<256, 512, G1_LDS, stream>>>(x, wqkv, wg, wu, gate_w, ws, wd, wo, (const u16*)d_out);
  k2_fused<<<256, 512, K2_LDS, stream>>>(wd, wo, ws, (u16*)d_out);
}
```

```cpp
#include <hip/hip_runtime.h>
#include <hip/hip_bf16.h>
#include <stdint.h>

#define LAS __attribute__((address_space(3)))
typedef unsigned short u16;
typedef short bf16x8 __attribute__((ext_vector_type(8)));
typedef short s16x4 __attribute__((ext_vector_type(4)));
typedef float f32x4 __attribute__((ext_vector_type(4)));
typedef float f32x16 __attribute__((ext_vector_type(16)));
typedef unsigned u32x4 __attribute__((ext_vector_type(4)));
typedef unsigned u32x2 __attribute__((ext_vector_type(2)));

constexpr int S_ = 2048, D_ = 1024, F_ = 3584, NQKV = 3072, K2 = F_ + D_;
constexpr float QSCALE = 0.17677669529663687f * 1.4426950408889634f;

constexpr size_t WS_QKV = 0;
constexpr size_t WS_A2 = WS_QKV + (size_t)NQKV * S_ * 2;
constexpr size_t WS_G = WS_A2 + (size_t)K2 * S_ * 2;
constexpr size_t WS_CNT = WS_G + S_ * 4;
constexpr size_t WS_PART = WS_CNT + 65536;

#define WAIT_V(n) asm volatile("s_waitcnt vmcnt(" #n ")" ::: "memory")
#define WAIT_L0 asm volatile("s_waitcnt lgkmcnt(0)" ::: "memory")
#define BAR __builtin_amdgcn_s_barrier()
#define SCHED __builtin_amdgcn_sched_barrier(0)

#define STAMP_DECL do {} while (0)
#define STAMP(kid, i) do {} while (0)
#define STAMP_END(kid, n) do {} while (0)
__device__ __forceinline__ int fresh_lane() {
  int l;
  asm volatile("v_mbcnt_lo_u32_b32 %0, -1, 0\n\tv_mbcnt_hi_u32_b32 %0, -1, %0" : "=v"(l));
  return l;
}
__device__ __forceinline__ void glds16(const void* sbase, unsigned voff, unsigned lds_addr) {
  asm volatile("s_mov_b32 m0, %2\n\ts_nop 0\n\tglobal_load_lds_dwordx4 %0, %1" ::"v"(voff), "s"(sbase), "s"(lds_addr) : "memory");
}
typedef __bf16 bf16x2_t __attribute__((ext_vector_type(2)));
typedef float f32x2 __attribute__((ext_vector_type(2)));
__device__ __forceinline__ unsigned cvtpk(float lo, float hi) {
  return __builtin_bit_cast(unsigned, __builtin_convertvector((f32x2){lo, hi}, bf16x2_t));
}
__device__ __forceinline__ bf16x8 lds_b128(const char LAS* p) { return *(const LAS bf16x8*)p; }
__device__ __forceinline__ s16x4 lds_tr(const char LAS* p) {
  return __builtin_bit_cast(s16x4, __builtin_amdgcn_ds_read_tr16_b64_v4i16((LAS s16x4*)p));
}
__device__ __forceinline__ bf16x8 cat8(s16x4 a, s16x4 b) {
  return (bf16x8){a[0], a[1], a[2], a[3], b[0], b[1], b[2], b[3]};
}
__device__ __forceinline__ f32x2 geglu2(f32x2 x, f32x2 u) {
  const float c0 = -2.0f * 0.7978845608028654f * 1.4426950408889634f;
  const float c1 = c0 * 0.044715f;
  const f32x2 t = x * x;
  const f32x2 z = x * (t * c1 + c0);
  f32x2 e; e.x = __builtin_amdgcn_exp2f(z.x); e.y = __builtin_amdgcn_exp2f(z.y);
  const f32x2 d = e + 1.0f;
  f32x2 r; r.x = __builtin_amdgcn_rcpf(d.x); r.y = __builtin_amdgcn_rcpf(d.y);
  return (x * u) * r;
}
__device__ __forceinline__ void glds4(const void* sbase, unsigned voff, unsigned lds_addr) {
  asm volatile("s_mov_b32 m0, %2\n\ts_nop 0\n\tglobal_load_lds_dword %0, %1" ::"v"(voff), "s"(sbase), "s"(lds_addr) : "memory");
}

constexpr int K2_SLOT = 24576;
constexpr int K2_G = 6 * K2_SLOT;
constexpr int K2_LDS = K2_G + 1024;
constexpr int AT_K = 3 * K2_SLOT;
constexpr int AT_V = AT_K + 32768;
constexpr int AT_V1 = AT_V + 16384 + 128;
static_assert(AT_V1 + 16384 <= K2_LDS, "lds");
#define NO_ 0
constexpr int NH = 28, NO = NO_, NK = NH + NO, NC = 16 - 2 * NO, NS = NK + NC;

__device__ __forceinline__ void st16_sc1(void* p, u32x4 w) {
  asm volatile("global_store_dwordx4 %0, %1, off sc1\n\ts_nop 1" ::"v"(p), "v"(w) : "memory");
}
template <int SC1>
__device__ __forceinline__ void glds16x4(const void* sbase, unsigned voff, unsigned lds_addr) {
  if (SC1)
    asm volatile("s_mov_b32 m0, %2\n\ts_nop 0\n\tglobal_load_lds_dwordx4 %0, %1 sc1\n\tglobal_load_lds_dwordx4 %0, %1 offset:1024 sc1\n\t"
                 "global_load_lds_dwordx4 %0, %1 offset:2048 sc1\n\tglobal_load_lds_dwordx4 %0, %1 offset:3072 sc1" ::"v"(voff), "s"(sbase), "s"(lds_addr) : "memory");
  else
    asm volatile("s_mov_b32 m0, %2\n\ts_nop 0\n\tglobal_load_lds_dwordx4 %0, %1\n\tglobal_load_lds_dwordx4 %0, %1 offset:1024\n\t"
                 "global_load_lds_dwordx4 %0, %1 offset:2048\n\tglobal_load_lds_dwordx4 %0, %1 offset:3072" ::"v"(voff), "s"(sbase), "s"(lds_addr) : "memory");
}
__device__ __forceinline__ void glds16x2(const void* sbase, unsigned voff0, unsigned voff1m, unsigned lds_addr) {
  asm volatile("s_mov_b32 m0, %3\n\ts_nop 0\n\tglobal_load_lds_dwordx4 %0, %2\n\tglobal_load_lds_dwordx4 %1, %2 offset:1024" ::"v"(voff0), "v"(voff1m),
               "s"(sbase), "s"(lds_addr) : "memory");
}
#define LP(a) ((const char LAS*)(uintptr_t)(a))

__device__ __forceinline__ float vsub(float a, float b) { float r; asm("v_sub_f32 %0, %1, %2" : "=v"(r) : "v"(a), "v"(b)); return r; }
__device__ __forceinline__ float vmul(float a, float b) { float r; asm("v_mul_f32 %0, %1, %2" : "=v"(r) : "v"(a), "v"(b)); return r; }
template <bool FIRST, class KW, class VL, class VW, class PF>
__device__ __forceinline__ void attn_wave(const char LAS* lds, char* ws, int wid, int lane, int head, int qrow, const bf16x8 (&qf)[2], const float& gval,
                                          KW kwait, VL vload, VW vwait, PF pf) {
  const int r32 = lane & 31, hi = lane >> 5;
  const int kb0 = FIRST ? 8 - wid : 0;
  const int dq = r32 - 4 * hi;
  const char LAS* kbase = lds + AT_K + wid * 1024 + r32 * 32 + ((hi ^ ((r32 >> 3) & 1)) << 4);
  const f32x16 zero = {0.f, 0.f, 0.f, 0.f, 0.f, 0.f, 0.f, 0.f, 0.f, 0.f, 0.f, 0.f, 0.f, 0.f, 0.f, 0.f};
  bf16x8 kf[9][2];
  f32x16 sb[3];
#define QKA(kb, dst, basev) dst = __builtin_amdgcn_mfma_f32_32x32x16_bf16(kf[kb][0], qf[0], (kb) == 0 ? mk0 : ((kb) == 8 ? mk8 : basev), 0, 0, 0)
#define QKB(kb, dst) dst = __builtin_amdgcn_mfma_f32_32x32x16_bf16(kf[kb][1], qf[1], dst, 0, 0, 0)
#define MX3(a, b, c) __builtin_fmaxf(__builtin_fmaxf(a, b), c)
  f32x16 mk0, mk8;
#pragma unroll
  for (int r = 0; r < 16; ++r) { const bool ab = ((r & 3) + 8 * (r >> 2)) > dq; mk0[r] = ab ? 0.0f : -INFINITY; mk8[r] = ab ? -INFINITY : 0.0f; }
  bf16x8 sel;
  { const short one = (((lane >> 4) & 1) == (lane & 1)) ? (short)0x3F80 : (short)0;
    sel = (bf16x8){one, one, one, one, one, one, one, one}; }
  const char LAS* vbase;
  { const int g = lane >> 4, q = (lane & 15) >> 2, p = lane & 3;
    vbase = lds + ((g & 1) ? AT_V1 : AT_V) + wid * 1024 + (4 * (g >> 1) + q) * 32 + p * 8; }
  u16* optr = (u16*)(ws + WS_A2) + ((size_t)(224 + head * 2) * S_ + qrow) * 16 + hi * 8;
  asm volatile("" : "+v"(mk0), "+v"(mk8), "+v"(sel), "+v"(vbase), "+v"(kbase), "+v"(optr));
  kwait();
#pragma unroll
  for (int kb = 0; kb < 9; ++kb) { kf[kb][0] = lds_b128(kbase + kb * 1024); kf[kb][1] = lds_b128(kbase + 16384 + kb * 1024); }
  vload();
  float ma = -INFINITY, mb = -INFINITY;
  SCHED;
#pragma unroll
  for (int kb = 0; kb < 11; ++kb) {
    const int pb = kb - 2;
    float a_ = ma, b_ = mb;
    if (kb < 9) { QKA(kb, sb[kb % 3], zero); SCHED; }
    if (pb >= 0) { const f32x16& t_ = sb[pb % 3]; a_ = MX3(a_, t_[0], t_[1]); b_ = MX3(b_, t_[2], t_[3]); a_ = MX3(a_, t_[4], t_[5]); b_ = MX3(b_, t_[6], t_[7]); SCHED; }
    if (kb < 9) { QKB(kb, sb[kb % 3]); SCHED; }
    if (pb >= 0) { const f32x16& t_ = sb[pb % 3]; a_ = MX3(a_, t_[8], t_[9]); b_ = MX3(b_, t_[10], t_[11]); a_ = MX3(a_, t_[12], t_[13]); b_ = MX3(b_, t_[14], t_[15]);
      if (FIRST) { ma = pb >= kb0 ? a_ : ma; mb = pb >= kb0 ? b_ : mb; } else { ma = a_; mb = b_; } }
    SCHED;
  }
  float m = __builtin_fmaxf(ma, mb);
  { auto rr = __builtin_amdgcn_permlane32_swap(__float_as_uint(m), __float_as_uint(m), false, false);
    m = __builtin_fmaxf(__uint_as_float(rr[0]), __uint_as_float(rr[1])); }
  f32x16 cm;
#pragma unroll
  for (int r = 0; r < 16; ++r) mk0[r] = vsub(mk0[r], m);
  asm volatile("s_nop 1" : "+v"(mk0));
  QKA(0, sb[0], cm); SCHED;
#pragma unroll
  for (int r = 0; r < 16; ++r) cm[r] = -m;
  asm volatile("" : "+v"(cm));
  QKB(0, sb[0]); SCHED;
#pragma unroll
  for (int r = 0; r < 16; ++r) mk8[r] = vsub(mk8[r], m);
  vwait();
  f32x16 o = zero;
  f32x4 lsum = {0.f, 0.f, 0.f, 0.f};
  s16x4 vf[2][4]; unsigned pk[8]; float ex[2][2];
#define SB2(kb) sb[(kb) & 1]
#define VF(kb) do { _Pragma("unroll") for (int i_ = 0; i_ < 4; ++i_) vf[(kb) & 1][i_] = lds_tr(vbase + (kb) * 1024 + i_ * 256); } while (0)
#define PW(st) __builtin_bit_cast(bf16x8, (u32x4){pk[4 * (st)], pk[4 * (st) + 1], pk[4 * (st) + 2], pk[4 * (st) + 3]})
#define PVM(kb, st) do { if (!FIRST || (kb) >= kb0) o = __builtin_amdgcn_mfma_f32_32x32x16_bf16(cat8(vf[(kb) & 1][2 * (st)], vf[(kb) & 1][2 * (st) + 1]), PW(st), o, 0, 0, 0); } while (0)
#define SUM(kb, st) do { if (!FIRST || (kb) >= kb0) lsum = __builtin_amdgcn_mfma_f32_16x16x32_bf16(sel, PW(st), lsum, 0, 0, 0); } while (0)
  VF(0);
#pragma unroll
  for (int kb = 0; kb < 9; ++kb) {
#pragma unroll
    for (int j = 0; j < 8; ++j) {
      ex[j & 1][0] = __builtin_amdgcn_exp2f(SB2(kb)[2 * j]); ex[j & 1][1] = __builtin_amdgcn_exp2f(SB2(kb)[2 * j + 1]);
      asm volatile("" : "+v"(ex[j & 1][0]), "+v"(ex[j & 1][1])); SCHED;
      if (kb + j > 0) { pk[(j + 7) & 7] = cvtpk(ex[(j + 1) & 1][0], ex[(j + 1) & 1][1]); asm volatile("" : "+v"(pk[(j + 7) & 7])); }
      if (j == 0 && kb + 1 < 9) QKA(kb + 1, SB2(kb + 1), cm);
      if (j == 1 && kb > 0) PVM(kb - 1, 1);
      if (j == 2 && kb > 0) SUM(kb - 1, 1);
      if (j == 3 && kb + 1 < 9) QKB(kb + 1, SB2(kb + 1));
      if (j == 3 && kb + 1 < 9) VF(kb + 1);
      if (j == 5) PVM(kb, 0);
      if (j == 6) pf(kb);
      if (j == 7) SUM(kb, 0);
      SCHED;
    }
  }
  pk[7] = cvtpk(ex[1][0], ex[1][1]);
  PVM(8, 1); SUM(8, 1);
  const float l = (lane & 16) ? lsum[1] : lsum[0];
  float sc = gval * __builtin_amdgcn_rcpf(l);
  { float o0_ = o[0] + 0.0f; asm volatile("s_nop 7\n\ts_nop 7" : "+v"(sc), "+v"(o0_)); }
#pragma unroll
  for (int dh = 0; dh < 2; ++dh) {
    const unsigned a0 = cvtpk(vmul(o[8 * dh + 0], sc), vmul(o[8 * dh + 1], sc)), a1 = cvtpk(vmul(o[8 * dh + 2], sc), vmul(o[8 * dh + 3], sc));
    const unsigned b0 = cvtpk(vmul(o[8 * dh + 4], sc), vmul(o[8 * dh + 5], sc)), b1 = cvtpk(vmul(o[8 * dh + 6], sc), vmul(o[8 * dh + 7], sc));
    auto r0 = __builtin_amdgcn_permlane32_swap(a0, b0, false, false);
    auto r1 = __builtin_amdgcn_permlane32_swap(a1, b1, false, false);
    u32x4 w = {r0[0], r1[0], r0[1], r1[1]};
    st16_sc1(optr + (size_t)dh * S_ * 16, w);
  }
#undef QKA
#undef QKB
#undef VF
#undef PW
#undef PVM
#undef SUM
#undef SB2
}

__device__ __forceinline__ void wait_count(unsigned* c, unsigned want) {
  unsigned spins = 0;
  while ((unsigned)__builtin_amdgcn_readfirstlane(__hip_atomic_load(c, __ATOMIC_RELAXED, __HIP_MEMORY_SCOPE_AGENT)) < want) {
    __builtin_amdgcn_s_sleep(4);
    if (++spins > (1u << 21)) break;
  }
  asm volatile("" ::: "memory");
}

__global__ __launch_bounds__(512, 2) void k2_fused(const u16* __restrict__ wd, const u16* __restrict__ wo, char* __restrict__ ws,
                                                   u16* __restrict__ out) {
  extern __shared__ __attribute__((aligned(16))) char lds_[];
  char LAS* lds = (char LAS*)lds_;
  const unsigned lds0 = (unsigned)(uintptr_t)lds;
  const int tid = threadIdx.x, lane = tid & 63;
  const int wid = __builtin_amdgcn_readfirstlane(tid >> 6);
  const int bid = blockIdx.x;
  const int xcd = bid & 7, idx = bid >> 3;
  const int sp = xcd >> 1, tn = idx & 7, tm = (xcd & 1) * 4 + (idx >> 3);
  const int mp = sp >> 1, np = sp & 1;
  const int tile = tm * 8 + tn;
  const int head = sp * 8 + tn, rb = tm;
  const int q0 = rb * 256;
  const char* qkv = ws + WS_QKV;
  unsigned* cntA = (unsigned*)(ws + WS_CNT);
  unsigned* cntP = (unsigned*)(ws + WS_CNT + 16 * 128);
  STAMP_DECL;
  STAMP(1, 0);

  const int qrow = q0 + wid * 32 + (lane & 31);
  const int rowlo = (rb > 0 ? q0 - 256 : 0) + wid * 32, rowhi = q0 + wid * 32;
  bf16x8 qf[2];
  float gval;
  {
    const char* qp = qkv + ((size_t)(head * 2) * S_ + q0 + wid * 32) * 32;
    const unsigned voffQ = (lane & 31) * 32 + (lane >> 5) * 16;
    qf[0] = *(const bf16x8*)(qp + voffQ);
    qf[1] = *(const bf16x8*)(qp + S_ * 32 + voffQ);
    gval = ((const float*)(ws + WS_G))[qrow];
    const unsigned voffK = (lane >> 1) * 32 + (((lane & 1) ^ ((lane >> 4) & 1)) << 4);

#pragma unroll
    for (int dh = 0; dh < 2; ++dh) {
      glds16(qkv + ((size_t)(64 + head * 2 + dh) * S_ + rowhi) * 32, voffK, lds0 + AT_K + dh * 16384 + (wid + 8) * 1024);
      glds16(qkv + ((size_t)(64 + head * 2 + dh) * S_ + rowlo) * 32, voffK, lds0 + AT_K + dh * 16384 + wid * 1024);
    }
  }

  const int ld = wid & 3;
  unsigned voffA, voffB0, voffB1m;
  auto set_voff = [&]() {
    const int l_ = fresh_lane();
    const int r = l_ >> 3, j = l_ & 7, q = r & 3;
    const int f0 = (q >> 1) & 1, f1 = f0 | 2;
    voffA = l_ * 16;
    voffB0 = r * 2048 + ((((j >> 1) ^ f0) << 1 | (j & 1)) << 4);
    voffB1m = (r + 8) * 2048 + ((((j >> 1) ^ f1) << 1 | (j & 1)) << 4) - 1024;
  };
  const char* hA = ws + WS_A2 + ((size_t)(sp * NH * 2 + (ld >> 1)) * S_ + tm * 256 + ((ld & 1) ^ mp) * 128) * 32;
  const char* hB = (const char*)wd + (size_t)(sp * NH * 32 + 16 * (ld & 1)) * 2048 + (size_t)(tn * 128 + ((ld >> 1) ^ np) * 64) * 2;
  const char* oA = ws + WS_A2 + ((size_t)(224 + sp * NO * 2 + (ld >> 1)) * S_ + tm * 256 + ((ld & 1) ^ mp) * 128) * 32;
  const char* oB = (const char*)wo + (size_t)(sp * NO * 32 + 16 * (ld & 1)) * 2048 + (size_t)(tn * 128 + ((ld >> 1) ^ np) * 64) * 2;
  const char* cA = ws + WS_A2 + ((size_t)(224 + 8 * NO + ld) * S_ + tm * 256 + mp * 128) * 32;
  const char* cB = (const char*)wo + (size_t)(128 * NO + 16 * ld) * 2048 + (size_t)(tn * 128 + np * 64) * 2;
  auto issue = [&](int i) {
    const unsigned dst = lds0 + (i % 6) * K2_SLOT;
    if (i < NH) {
      glds16x4<0>(hA + (size_t)i * (2 * S_ * 32), voffA, dst + ld * 4096);
      glds16x2(hB + (size_t)i * (32 * 2048), voffB0, voffB1m, dst + 16384 + ld * 2048);
    } else if (i < NK) {
      glds16x4<1>(oA + (size_t)(i - NH) * (2 * S_ * 32), voffA, dst + ld * 4096);
      glds16x2(oB + (size_t)(i - NH) * (32 * 2048), voffB0, voffB1m, dst + 16384 + ld * 2048);
    } else {
      glds16x4<1>(cA + (size_t)(i - NK) * (4 * S_ * 32), voffA, dst + ld * 4096);
      glds16x2(cB + (size_t)(i - NK) * (64 * 2048), voffB0, voffB1m, dst + 16384 + ld * 2048);
    }
  };

  {
    set_voff();
    const int hf = wid >> 2;
    unsigned voffBp = hf ? voffB1m + 1024 : voffB0;
    const char* pA = hA + hf * 2048;
    const unsigned dA = lds0 + ld * 4096 + hf * 2048, dB = lds0 + 16384 + ld * 2048 + hf * 1024;
    auto kwait = [&]() {
      asm volatile("" : "+v"(voffA), "+v"(voffBp));
      asm volatile("s_waitcnt vmcnt(0)" : "+v"(qf[0]), "+v"(qf[1]), "+v"(gval) :: "memory");
      BAR;
      asm volatile("" ::: "memory");
      STAMP(1, 1);
    };
    auto vload = [&]() {
#pragma unroll
      for (int dh = 0; dh < 2; ++dh) {
        glds16(qkv + ((size_t)(128 + head * 2 + dh) * S_ + rowlo) * 32, voffA, lds0 + (dh ? AT_V1 : AT_V) + wid * 1024);
        glds16(qkv + ((size_t)(128 + head * 2 + dh) * S_ + rowhi) * 32, voffA, lds0 + (dh ? AT_V1 : AT_V) + (wid + 8) * 1024);
      }
      if (lane < 32) *(LAS float*)(lds + K2_G + (wid * 32 + lane) * 4) = 1.0f - gval;
    };
    auto vwait = [&]() { WAIT_V(0); BAR; asm volatile("" ::: "memory"); };
    auto pf = [&](int kb) {
      const int st = kb / 3, j = kb % 3;
      if (j < 2) glds16(pA + (size_t)st * (2 * S_ * 32) + j * 1024, voffA, dA + st * K2_SLOT + j * 1024);
      else glds16(hB + (size_t)st * (32 * 2048), voffBp, dB + st * K2_SLOT);
    };
    if (rb == 0) attn_wave<true>(lds, ws, wid, lane, head, qrow, qf, gval, kwait, vload, vwait, pf);
    else attn_wave<false>(lds, ws, wid, lane, head, qrow, qf, gval, kwait, vload, vwait, pf);
  }
  STAMP(1, 2);

  if (wid >= 4) {
    set_voff();
    WAIT_V(8);
    WAIT_L0; BAR;
    issue(3); issue(4);
    WAIT_V(14);
    BAR;
    unsigned* ca = cntA + tm * 32;
    unsigned fl = 0;
    for (int P = 0; P < NS / 2; ++P) {
      const int a = 2 * P + 5;
      if (P == 9) asm volatile("global_load_dword %0, %1, off sc1" : "=v"(fl) : "v"(ca) : "memory");
      if (P == 11) {
        asm volatile("" : "+v"(fl));
        if ((unsigned)__builtin_amdgcn_readfirstlane(fl) < 256u) wait_count(ca, 256u);
      }
      if (a + 1 < NS) { issue(a); issue(a + 1); WAIT_V(12); }
      else if (a < NS) { issue(a); WAIT_V(6); }
      else { WAIT_V(0); }
      if (P == 0 && voffA == 0) __hip_atomic_fetch_add(ca, 1u, __ATOMIC_RELAXED, __HIP_MEMORY_SCOPE_AGENT);
      if (P + 1 < NS / 2) BAR;
    }
    STAMP(1, 3); STAMP(1, 4); STAMP(1, 5); STAMP(1, 6);
    STAMP_END(1, 7);
    return;
  }

  const int wm = wid & 1, wn = wid >> 1;
  const int fr = lane & 15, g4 = lane >> 4;
  const unsigned rAh = lds0 + (g4 >> 1) * 8192 + (wm * 64 + fr) * 32 + (g4 & 1) * 16;
  unsigned rAc = 0;
  unsigned rB0, rB1;
  { const int q = (lane & 15) >> 2, p = lane & 3;
    const int f = ((q >> 1) & 1) | ((g4 & 1) << 1);
    rB0 = lds0 + 16384 + g4 * 1024 + q * 128 + (((wn * 2) ^ f) << 5) + p * 8;
    rB1 = rB0 ^ 32u; }

  f32x4 acc[8][4];
#pragma unroll
  for (int m = 0; m < 8; ++m)
#pragma unroll
    for (int n = 0; n < 4; ++n) { acc[m][n] = (f32x4){0.f, 0.f, 0.f, 0.f}; asm volatile("" : "+v"(acc[m][n])); }
  bf16x8 A0[8], A1[8];
  s16x4 L0[4], H0[4], L1[4], H1[4];
#define RD(AF, BL, BH, k, so, COV) do { \
    if ((k) < 8) { const int n_ = (k) >> 1; const unsigned b_ = ((n_ & 1) ? rB1 : rB0) + (so); \
      if ((k) & 1) BH[n_] = lds_tr(LP(b_) + (n_ >> 1) * 4096 + 512); else BL[n_] = lds_tr(LP(b_) + (n_ >> 1) * 4096); } \
    else { const int m_ = (k) - 8; \
      if (COV) AF[m_] = lds_b128(LP(rAc + (so)) + (m_ >> 2) * 8192 + (m_ & 3) * 512); \
      else AF[m_] = lds_b128(LP(rAh + (so)) + (m_ >> 2) * 4096 + (m_ & 3) * 512); } } while (0)
#define MFH(AF, BL, BH, k) acc[(k) >> 2][(k) & 3] = __builtin_amdgcn_mfma_f32_16x16x32_bf16(cat8(BL[(k) & 3], BH[(k) & 3]), AF[(k) >> 2], acc[(k) >> 2][(k) & 3], 0, 0, 0)
#define STAGE_H(AC, LC, HC, AN, LN, HN, so, COV) do { \
    _Pragma("unroll") for (int k = 0; k < 32; ++k) { MFH(AC, LC, HC, k); if (k >= 1 && k < 17) RD(AN, LN, HN, k - 1, so, COV); SCHED; } } while (0)

  WAIT_V(8); WAIT_L0; BAR;
  asm volatile("" ::: "memory");
#pragma unroll
  for (int k = 0; k < 16; ++k) RD(A0, L0, H0, k, 0, 0);
  WAIT_V(2); WAIT_L0; BAR;
  SCHED;
  STAMP(1, 3);
  int s1 = 1;
#pragma unroll 1
  for (int ph = 0; ph < 2; ++ph) {
    const int np_ = ph ? (NO > 0 ? NO / 2 - 1 : 0) : (NO > 0 ? NH / 2 : NH / 2 - 1);
#pragma unroll 1
    for (int P = 0; P < np_; ++P) {
      const int s2 = (s1 == 5) ? 0 : s1 + 1;
      STAGE_H(A0, L0, H0, A1, L1, H1, s1 * K2_SLOT, 0);
      STAGE_H(A1, L1, H1, A0, L0, H0, s2 * K2_SLOT, 0);
      s1 = s2 + 1;
      if (P == 2) { WAIT_V(0); if (fresh_lane() == 0) __hip_atomic_fetch_add(cntA + tm * 32, 1u, __ATOMIC_RELAXED, __HIP_MEMORY_SCOPE_AGENT); }
      WAIT_L0; BAR; SCHED;
    }
    if (ph == 0 && NO > 0) {
      const int l4 = fresh_lane();
#pragma unroll
      for (int m = 0; m < 8; ++m) {
        const float sc = *(const LAS float*)(lds + K2_G + (((m >> 2) ^ mp) * 128 + wm * 64 + (m & 3) * 16 + (l4 & 15)) * 4);
#pragma unroll
        for (int n = 0; n < 4; ++n) acc[m][n] *= sc;
      }
      SCHED;
    }
  }
  STAGE_H(A0, L0, H0, A1, L1, H1, ((NK - 1) % 6) * K2_SLOT, 0);
  { const int l2 = fresh_lane(); rAc = lds0 + (l2 >> 5) * 4096 + (wm * 64 + (l2 & 15)) * 32 + ((l2 >> 4) & 1) * 16; }
  STAGE_H(A1, L1, H1, A0, L0, H0, (NK % 6) * K2_SLOT, 1);
  WAIT_L0; BAR; SCHED;
  STAMP(1, 4);

  auto rs = __builtin_amdgcn_make_buffer_rsrc((void*)(ws + WS_PART), 0, 16u << 20, 0x00020000);
  const int l3 = fresh_lane();
  if (NO == 0) {
#pragma unroll
    for (int m = 0; m < 8; ++m) {
      const float sc = *(const LAS float*)(lds + K2_G + (((m >> 2) ^ mp) * 128 + wm * 64 + (m & 3) * 16 + (l3 & 15)) * 4);
#pragma unroll
      for (int n = 0; n < 4; ++n) acc[m][n] *= sc;
    }
  }
#pragma unroll
  for (int d = 3; d >= 1; --d) {
    const int h = d >> 1, c = d & 1;
#pragma unroll
    for (int mb = 0; mb < 4; ++mb) {
      const f32x4 u = acc[4 * h + mb][2 * c], v = acc[4 * h + mb][2 * c + 1];
      u32x4 w = {cvtpk(u[0], u[1]), cvtpk(u[2], u[3]), cvtpk(v[0], v[1]), cvtpk(v[2], v[3])};
      const unsigned off = (((((unsigned)tile * 4 + (d ^ sp)) * 4 + sp) * 4 + wid) * 4 + mb) * 1024u + l3 * 16;
      __builtin_amdgcn_raw_buffer_store_b128(w, rs, off, 0, 16);
    }
  }
  f32x4 acK[4][2];
#pragma unroll
  for (int mb = 0; mb < 4; ++mb)
#pragma unroll
    for (int j = 0; j < 2; ++j) acK[mb][j] = acc[mb][j];
  SCHED;
#define MFC(AF, BL, BH, k) acK[((k) >> 1) & 3][(k) & 1] = __builtin_amdgcn_mfma_f32_16x16x32_bf16(cat8(BL[2 * ((k) >> 3) + ((k) & 1)], BH[2 * ((k) >> 3) + ((k) & 1)]), \
                                                             AF[4 * ((k) >> 3) + (((k) >> 1) & 3)], acK[((k) >> 1) & 3][(k) & 1], 0, 0, 0)
#define STAGE_C(AC, LC, HC, AN, LN, HN, so) do { \
    _Pragma("unroll") for (int k = 0; k < 16; ++k) { MFC(AC, LC, HC, k); RD(AN, LN, HN, k, so, 1); SCHED; } } while (0)
  u32x4 part[3][4];
  unsigned* myc = cntP + (tile * 4 + wid) * 32;
  unsigned flagv = 0;
#define XSIG 2
#define XLD 2
#pragma unroll
  for (int P = NK / 2; P < NS / 2; ++P) {
    STAGE_C(A0, L0, H0, A1, L1, H1, ((2 * P + 1) % 6) * K2_SLOT);
    if (P + 1 < NS / 2) { STAGE_C(A1, L1, H1, A0, L0, H0, ((2 * P + 2) % 6) * K2_SLOT); }
    else {
#pragma unroll
      for (int k = 0; k < 16; ++k) MFC(A1, L1, H1, k);
    }
    if (P == NK / 2 + XSIG) { WAIT_V(0); if (l3 == 0) __hip_atomic_fetch_add(myc, 1u, __ATOMIC_RELAXED, __HIP_MEMORY_SCOPE_AGENT); }
    if (P == NS / 2 - 2 - XLD) flagv = __hip_atomic_load(myc, __ATOMIC_RELAXED, __HIP_MEMORY_SCOPE_AGENT);
    if (P + 1 < NS / 2) { WAIT_L0; BAR; SCHED; }
    if (P == NS / 2 - 1 - XLD) {
      if ((unsigned)__builtin_amdgcn_readfirstlane(flagv) < 4u) wait_count(myc, 4u);
      asm volatile("" ::: "memory");
#pragma unroll
      for (int jj = 0; jj < 3; ++jj) {
        const int src = (sp + 1 + jj) & 3;
#pragma unroll
        for (int mb = 0; mb < 4; ++mb) {
          const unsigned off = (((((unsigned)tile * 4 + sp) * 4 + src) * 4 + wid) * 4 + mb) * 1024u + l3 * 16;
          part[jj][mb] = __builtin_amdgcn_raw_buffer_load_b128(rs, off, 0, 16);
        }
      }
    }
  }
  STAMP(1, 5);
  const int lane_e = fresh_lane();
#pragma unroll
  for (int mb = 0; mb < 4; ++mb) {
    f32x4 u = acK[mb][0], v = acK[mb][1];
#pragma unroll
    for (int jj = 0; jj < 3; ++jj) {
      const u32x4 w = part[jj][mb];
      u[0] += __uint_as_float(w[0] << 16); u[1] += __uint_as_float(w[0] & 0xffff0000u);
      u[2] += __uint_as_float(w[1] << 16); u[3] += __uint_as_float(w[1] & 0xffff0000u);
      v[0] += __uint_as_float(w[2] << 16); v[1] += __uint_as_float(w[2] & 0xffff0000u);
      v[2] += __uint_as_float(w[3] << 16); v[3] += __uint_as_float(w[3] & 0xffff0000u);
    }
    auto r0 = __builtin_amdgcn_permlane16_swap(cvtpk(u[0], u[1]), cvtpk(v[0], v[1]), false, false);
    auto r1 = __builtin_amdgcn_permlane16_swap(cvtpk(u[2], u[3]), cvtpk(v[2], v[3]), false, false);
    u32x4 w = {r0[0], r1[0], r0[1], r1[1]};
    const int row = tm * 256 + mp * 128 + wm * 64 + mb * 16 + (lane_e & 15);
    const int col = tn * 128 + np * 64 + wn * 32 + ((lane_e >> 4) & 1) * 16 + (lane_e >> 5) * 8;
    *(u32x4*)(out + (size_t)row * D_ + col) = w;
  }
  STAMP(1, 6);
  STAMP_END(1, 7);
}

constexpr int G1_LDS_A = 0;
constexpr int G1_LDS_B = 65536;
constexpr int G1_LDS_B4 = 131072;
constexpr int G1_LDS_GW = 147456;
constexpr int G1_LDS_DUMMY = G1_LDS_GW + 2048;
constexpr int G1_LDS = G1_LDS_DUMMY + 2048;

#define QMODE 2
#define HMODE 2
#define TBACK 0
template <int MODE, int OFF>
__device__ __forceinline__ void store_pair16(const void* sbase, unsigned voff, unsigned x0, unsigned x1, unsigned y0, unsigned y1) {
  auto r0 = __builtin_amdgcn_permlane16_swap(x0, y0, false, false);
  auto r1 = __builtin_amdgcn_permlane16_swap(x1, y1, false, false);
  u32x4 w = {r0[0], r1[0], r0[1], r1[1]};
  if (MODE == 0) asm volatile("global_store_dwordx4 %0, %1, %2 offset:%3\n\ts_nop 1" ::"v"(voff), "v"(w), "s"(sbase), "n"(OFF) : "memory");
  else if (MODE == 1) asm volatile("global_store_dwordx4 %0, %1, %2 offset:%3 nt\n\ts_nop 1" ::"v"(voff), "v"(w), "s"(sbase), "n"(OFF) : "memory");
  else asm volatile("global_store_dwordx4 %0, %1, %2 offset:%3 sc1\n\ts_nop 1" ::"v"(voff), "v"(w), "s"(sbase), "n"(OFF) : "memory");
}

__global__ __launch_bounds__(512, 2) void k_gemm1(const u16* __restrict__ x, const u16* __restrict__ wqkv, const u16* __restrict__ wg,
                                                  const u16* __restrict__ wu, const u16* __restrict__ gate_w, char* __restrict__ ws,
                                                  const u16* __restrict__ wd, const u16* __restrict__ wo) {
  extern __shared__ __attribute__((aligned(16))) char lds_[];
  char LAS* lds = (char LAS*)lds_;
  const unsigned lds0 = (unsigned)(uintptr_t)lds;
  const int tid = threadIdx.x, lane = tid & 63;
  const int wid = __builtin_amdgcn_readfirstlane(tid >> 6);
  const int wr = wid >> 2, wc = wid & 3;
  const int bid = blockIdx.x;
  const int xcd = bid & 7, idx = bid >> 3;
  const int sp = xcd >> 1, tm = (xcd & 1) * 4 + (idx >> 3), ct = idx & 7;
  const int tn = sp * 8 + ct;

  int ptype[5], pcol[5];
  {
    auto qc = [&](int i) { return (i >> 2) * 1024 + 256 * sp + 64 * (i & 3); };
    if (ct < 6) {
      ptype[0] = 1; ptype[1] = 2; pcol[0] = pcol[1] = 896 * sp + 128 * ct;
      ptype[2] = 1; ptype[3] = 2; pcol[2] = pcol[3] = 896 * sp + 128 * ct + 64;
      ptype[4] = 0; pcol[4] = qc(ct);
    } else {
      ptype[0] = 1; ptype[1] = 2; pcol[0] = pcol[1] = 896 * sp + 768 + 64 * (ct - 6);
      ptype[2] = ptype[3] = ptype[4] = 0;
      pcol[2] = qc(6 + 3 * (ct - 6)); pcol[3] = qc(7 + 3 * (ct - 6)); pcol[4] = qc(8 + 3 * (ct - 6));
    }
  }

  const char* sA = (const char*)x + (size_t)(tm * 256 + wid * 8) * 2048;
  unsigned voffA;
  { const int r = lane >> 3, j = lane & 7; voffA = r * 2048 + ((j ^ ((4 * (wid & 1) + (r >> 1)) & 7)) << 4); }
  const unsigned ldsA = lds0 + G1_LDS_A + wid * 1024;
  const int myp = wid >> 1;
  int mt = ptype[0], mc = pcol[0];
#pragma unroll
  for (int p = 1; p < 4; ++p) if (myp == p) { mt = ptype[p]; mc = pcol[p]; }
  const unsigned strB = (mt == 0) ? 6144u : 7168u;
  const char* sB = (const char*)(mt == 0 ? wqkv : (mt == 1 ? wg : wu)) + (size_t)mc * 2 + (size_t)(16 * (wid & 1)) * strB;
  unsigned voffB0, voffB1;
  { const int r = lane >> 3, j = lane & 7, q = r & 3;
    const int f0 = (q >> 1) & 1, f1 = f0 | 2;
    voffB0 = r * strB + ((((j >> 1) ^ f0) << 1 | (j & 1)) << 4);
    voffB1 = (r + 8) * strB + ((((j >> 1) ^ f1) << 1 | (j & 1)) << 4); }
  const unsigned ldsB = lds0 + G1_LDS_B + myp * 8192 + (wid & 1) * 2048;
  constexpr unsigned strB4 = 6144u;
  const char* sB4 = (const char*)wqkv + (size_t)pcol[4] * 2 + (size_t)(8 * wid) * strB4;
  unsigned voffB4;
  { const int r = lane >> 3, j = lane & 7, q = r & 3;
    const int f = ((q >> 1) & 1) | ((wid & 1) << 1);
    voffB4 = r * strB4 + ((((j >> 1) ^ f) << 1 | (j & 1)) << 4); }
  const unsigned ldsB4 = lds0 + G1_LDS_B4 + wid * 1024;

#define KC(kt) ((unsigned)((kt) < 15 ? (kt) : 15))
#define G1_A0(kt, b) do { const char* _p = sA + KC(kt) * 128u; glds16(_p, voffA, ldsA + (b) * 32768); \
                          glds16(_p + 64 * 2048, voffA, ldsA + (b) * 32768 + 8192); } while (0)
#define G1_A1(kt, b) do { const char* _p = sA + KC(kt) * 128u; glds16(_p + 128 * 2048, voffA, ldsA + (b) * 32768 + 16384); \
                          glds16(_p + 192 * 2048, voffA, ldsA + (b) * 32768 + 24576); } while (0)
#define G1_X(kt, b) do { const char* _p = sB + KC(kt) * 64u * strB; glds16(_p, voffB0, ldsB + (b) * 32768); \
                         glds16(_p, voffB1, ldsB + (b) * 32768 + 1024); } while (0)
#define G1_Y(kt, b) do { const char* _p = sB + (KC(kt) * 64u + 32u) * strB; glds16(_p, voffB0, ldsB + (b) * 32768 + 4096); \
                         glds16(_p, voffB1, ldsB + (b) * 32768 + 4096 + 1024); } while (0)
#define G1_Z(kt, b) glds16(sB4 + KC(kt) * 64u * strB4, voffB4, ldsB4 + (b) * 8192)

  STAMP_DECL;
  STAMP(0, 0);
  glds4((const char*)gate_w + wid * 256, lane * 4, lds0 + G1_LDS_GW + wid * 256);
  G1_X(0, 0); G1_A0(0, 0); G1_A1(0, 0); G1_Z(0, 0); G1_Y(0, 0); G1_X(1, 1);
#define TOUCH 1
#define TD 2
#define TPRO 1
#define CODEPF 1
  const char* tp; unsigned voffT, tstep, tlead, tmax = 15;
  if (wid == 0) { tp = (const char*)x + (size_t)(tm * 256 + ct * 32) * 2048; voffT = (lane & 31) * 2048; tstep = 128; tlead = TD; }
  else if (wid <= 5) {
    int t_ = ptype[0], c_ = pcol[0];
#pragma unroll
    for (int p = 1; p < 5; ++p) if (wid - 1 == p) { t_ = ptype[p]; c_ = pcol[p]; }
    const unsigned st_ = (t_ == 0) ? 6144u : 7168u;
    tp = (const char*)(t_ == 0 ? wqkv : (t_ == 1 ? wg : wu)) + (size_t)c_ * 2 + (size_t)((tm & 3) * 16) * st_;
    voffT = (lane & 15) * st_; tstep = 64 * st_; tlead = TD;
  } else if (wid == 6) { tp = (const char*)wd + (size_t)bid * 28672; voffT = (lane % 14) * 128; tstep = 1792; tlead = 1; }
  else if (idx >= 16 || !CODEPF) { tp = (const char*)wo + (size_t)(xcd * 16 + (idx & 15)) * 16384; voffT = (lane & 7) * 128; tstep = 1024; tlead = 1; }
  else {
    unsigned long long pc; int d;
    asm volatile("s_getpc_b64 %0\n\ts_mov_b32 %1, _Z8k2_fusedPKtS0_PcPt@rel32@lo+4" : "=s"(pc), "=s"(d));
    const bool ok = d < -4096;
    const unsigned n4k = ok ? (unsigned)(-d) / 4096u : 1u;
    tp = ok ? (const char*)(pc + (long long)d) : (const char*)wo;
    voffT = (lane & 31) * 128; tstep = 4096; tlead = (unsigned)-8; tmax = (n4k < 7u ? n4k : 7u) - 1u;
  }
  auto touch = [&](int j) { const unsigned k_ = (unsigned)j + tlead; glds4(tp + (k_ < tmax ? k_ : tmax) * tstep, voffT, lds0 + G1_LDS_DUMMY + wid * 256); };

  const int fr = lane & 15, g4 = lane >> 4;
  const char LAS* rA0 = lds + G1_LDS_A + wr * 8192 + fr * 128 + (((0 + g4) ^ (fr >> 1)) << 4);
  const char LAS* rA1 = lds + G1_LDS_A + wr * 8192 + fr * 128 + (((4 + g4) ^ (fr >> 1)) << 4);
  const char LAS* rB; const char LAS* rB4;
  { const int q = (lane & 15) >> 2, p = lane & 3;
    const int f = ((q >> 1) & 1) | ((g4 & 1) << 1);
    const int o = g4 * 1024 + q * 128 + ((wc ^ f) << 5) + p * 8;
    rB = lds + G1_LDS_B + o;
    rB4 = lds + G1_LDS_B4 + o; }

  const char LAS* rG; const char LAS* rGW;
  { const int r = tn * 8 + wid, l = lane & 31;
    rG = lds + G1_LDS_A + r * 128 + (((l >> 2) ^ ((r >> 1) & 7)) << 4) + (l & 3) * 4;
    rGW = lds + G1_LDS_GW + l * 4; }
  float gsum = 0.f;
  f32x4 acc[2][4][5];
#pragma unroll
  for (int a = 0; a < 2; ++a)
#pragma unroll
    for (int m = 0; m < 4; ++m)
#pragma unroll
      for (int p = 0; p < 5; ++p) { acc[a][m][p] = (f32x4){0.f, 0.f, 0.f, 0.f}; asm volatile("" : "+v"(acc[a][m][p])); }
  asm volatile("" : "+v"(rA0), "+v"(rA1), "+v"(rB), "+v"(rB4), "+v"(rG), "+v"(rGW), "+v"(voffT));
  bf16x8 Af[8], Bf[5];

#define G1_LDB(b, ks) do { _Pragma("unroll") for (int p = 0; p < 4; ++p) { \
      s16x4 lo = lds_tr(rB + (b) * 32768 + p * 8192 + (ks) * 4096), hi = lds_tr(rB + (b) * 32768 + p * 8192 + (ks) * 4096 + 512); Bf[p] = cat8(lo, hi); } \
    { s16x4 lo = lds_tr(rB4 + (b) * 8192 + (ks) * 4096), hi = lds_tr(rB4 + (b) * 8192 + (ks) * 4096 + 512); Bf[4] = cat8(lo, hi); } } while (0)
#define G1_LDA(b, ks) do { _Pragma("unroll") for (int m = 0; m < 8; ++m) \
      Af[m] = lds_b128(((ks) ? rA1 : rA0) + (b) * 32768 + (m >> 2) * 16384 + (m & 3) * 2048); } while (0)
#define EARLYB 0
#define G1_MMA() do { __builtin_amdgcn_s_setprio(1); _Pragma("unroll") for (int k = 0; k < 40; ++k) { const int m = k / 5, p = k % 5; \
      if (k == 40 - EARLYB) { SCHED; __builtin_amdgcn_s_setprio(3); BAR; SCHED; } \
      acc[m >> 2][m & 3][p] = __builtin_amdgcn_mfma_f32_16x16x32_bf16(Bf[p], Af[m], acc[m >> 2][m & 3][p], 0, 0, 0); } \
      __builtin_amdgcn_s_setprio(0); if (EARLYB == 0) BAR; } while (0)
#define WV_A WAIT_V(5)
#define WV_B WAIT_V(10)
  WAIT_V(4); BAR;
  STAMP(0, 1);
  if (TOUCH) {
#pragma unroll
    for (int e = TPRO; e >= 1; --e) touch(-e);
  }
  if (wr == 1) BAR;

#define G1_TILE(t, b) do { \
    G1_LDB(b, 0); G1_LDA(b, 0); \
    G1_A0((t) + 1, (b) ^ 1); G1_A1((t) + 1, (b) ^ 1); G1_Z((t) + 1, (b) ^ 1); G1_Y((t) + 1, (b) ^ 1); \
    WV_B; WAIT_L0; BAR; G1_MMA(); SCHED; \
    G1_LDB(b, 1); G1_LDA(b, 1); \
    { const unsigned xa = *(const LAS unsigned*)(rG + (b) * 32768), wa = *(const LAS unsigned*)(rGW + (t) * 128); \
      G1_X((t) + 2, b); if (TOUCH) touch(t); \
      WAIT_L0; \
      gsum = __builtin_fmaf(__uint_as_float(xa << 16), __uint_as_float(wa << 16), gsum); \
      gsum = __builtin_fmaf(__uint_as_float(xa & 0xffff0000u), __uint_as_float(wa & 0xffff0000u), gsum); \
      asm volatile("" : "+v"(gsum)); } \
    WV_A; BAR; G1_MMA(); SCHED; \
  } while (0)

#pragma unroll 1
  for (int t = 0; t < 16; t += 2) {
    G1_TILE(t, 0);
    G1_TILE(t + 1, 1);
  }
  STAMP(0, 2);
  const int lane_e = fresh_lane();
#define lane lane_e

  {
#pragma unroll
    for (int o = 16; o >= 1; o >>= 1) gsum += __shfl_xor(gsum, o);
    if (lane == 0) ((float*)(ws + WS_G))[tm * 256 + tn * 8 + wid] = 1.0f / (1.0f + __expf(-gsum));
    if (wid == 0 && lane == 0) ((unsigned*)(ws + WS_CNT))[(16 + bid) * 32] = 0u;
    if (wid == 1 && lane == 0 && bid < 16) ((unsigned*)(ws + WS_CNT))[bid * 32] = 0u;
  }

  const unsigned voffS = (((lane >> 4) & 1) * 16 + (lane & 15)) * 32 + (lane >> 5) * 16;
  const size_t rowoff = (size_t)(tm * 256 + wr * 64) * 32 + 2048;
#define G1_STQ(p) do { const float sc = pcol[p] < D_ ? QSCALE : 1.0f; \
    const char* dst = ws + WS_QKV + (size_t)(pcol[p] / 16 + wc) * (S_ * 32) + rowoff; \
    { const f32x4 u = acc[0][0][p] * sc, v = acc[0][1][p] * sc; store_pair16<QMODE, -2048>(dst, voffS, cvtpk(u[0], u[1]), cvtpk(u[2], u[3]), cvtpk(v[0], v[1]), cvtpk(v[2], v[3])); } \
    { const f32x4 u = acc[0][2][p] * sc, v = acc[0][3][p] * sc; store_pair16<QMODE, -1024>(dst, voffS, cvtpk(u[0], u[1]), cvtpk(u[2], u[3]), cvtpk(v[0], v[1]), cvtpk(v[2], v[3])); } \
    { const f32x4 u = acc[1][0][p] * sc, v = acc[1][1][p] * sc; store_pair16<QMODE, 2048>(dst, voffS, cvtpk(u[0], u[1]), cvtpk(u[2], u[3]), cvtpk(v[0], v[1]), cvtpk(v[2], v[3])); } \
    { const f32x4 u = acc[1][2][p] * sc, v = acc[1][3][p] * sc; store_pair16<QMODE, 3072>(dst, voffS, cvtpk(u[0], u[1]), cvtpk(u[2], u[3]), cvtpk(v[0], v[1]), cvtpk(v[2], v[3])); } } while (0)
#define G1_H1(pp, a, m, OFF) do { const f32x4 g0 = acc[a][m][2 * (pp)], u0 = acc[a][m][2 * (pp) + 1], g1 = acc[a][(m) + 1][2 * (pp)], u1 = acc[a][(m) + 1][2 * (pp) + 1]; \
    const f32x2 h0 = geglu2(g0.xy, u0.xy), h1 = geglu2(g0.zw, u0.zw), h2 = geglu2(g1.xy, u1.xy), h3 = geglu2(g1.zw, u1.zw); \
    store_pair16<HMODE, OFF>(dst, voffS, cvtpk(h0.x, h0.y), cvtpk(h1.x, h1.y), cvtpk(h2.x, h2.y), cvtpk(h3.x, h3.y)); } while (0)
#define G1_STH(pp) do { const char* dst = ws + WS_A2 + (size_t)(pcol[2 * (pp)] / 16 + wc) * (S_ * 32) + rowoff; \
    G1_H1(pp, 0, 0, -2048); G1_H1(pp, 0, 2, -1024); G1_H1(pp, 1, 0, 2048); G1_H1(pp, 1, 2, 3072); } while (0)
#define G1_TB(p) glds4(ws + WS_QKV + (size_t)(pcol[p] / 16 + wc) * (S_ * 32) + rowoff - 2048, voffTB, lds0 + G1_LDS_DUMMY + wid * 256)
  const unsigned voffTB = ((lane >> 4) & 1) * 4096 + (lane & 15) * 128;
  G1_STQ(4);
  if (wr == 0) BAR;
  WAIT_V(4);
  if (ptype[2] == 0) {
    G1_STQ(2); G1_STQ(3);
    G1_STH(0);
    if (TBACK) { WAIT_V(4); G1_TB(4); G1_TB(2); G1_TB(3); }
  } else {
    G1_STH(0);
    if (TBACK) { WAIT_V(4); G1_TB(4); }
    G1_STH(1);
  }
#undef lane
  STAMP(0, 3);
  STAMP(0, 4);
  STAMP_END(0, 5);
}

extern "C" void kernel_launch(void* const* d_in, const int* in_sizes, int n_in, void* d_out, int out_size, void* d_ws, size_t ws_size,
                              hipStream_t stream) {
  const u16* x = (const u16*)d_in[0];
  const u16* wqkv = (const u16*)d_in[1];
  const u16* wo = (const u16*)d_in[2];
  const u16* wg = (const u16*)d_in[4];
  const u16* wu = (const u16*)d_in[5];
  const u16* wd = (const u16*)d_in[6];
  const u16* gate_w = (const u16*)d_in[7];
  char* ws = (char*)d_ws;
  (void)hipFuncSetAttribute((const void*)k_gemm1, hipFuncAttributeMaxDynamicSharedMemorySize, G1_LDS);
  (void)hipFuncSetAttribute((const void*)k2_fused, hipFuncAttributeMaxDynamicSharedMemorySize, K2_LDS);
  k_gemm1<<<256, 512, G1_LDS, stream>>>(x, wqkv, wg, wu, gate_w, ws, wd, wo);
  k2_fused<<<256, 512, K2_LDS, stream>>>(wd, wo, ws, (u16*)d_out);
}
```

```cpp
#include <hip/hip_runtime.h>
#include <hip/hip_bf16.h>
#include <stdint.h>

#define LAS __attribute__((address_space(3)))
typedef unsigned short u16;
typedef short bf16x8 __attribute__((ext_vector_type(8)));
typedef short s16x4 __attribute__((ext_vector_type(4)));
typedef float f32x4 __attribute__((ext_vector_type(4)));
typedef float f32x16 __attribute__((ext_vector_type(16)));
typedef unsigned u32x4 __attribute__((ext_vector_type(4)));
typedef unsigned u32x2 __attribute__((ext_vector_type(2)));

constexpr int S_ = 2048, D_ = 1024, F_ = 3584, NQKV = 3072, K2 = F_ + D_;
constexpr float QSCALE = 0.17677669529663687f * 1.4426950408889634f;

constexpr size_t WS_QKV = 0;
constexpr size_t WS_A2 = WS_QKV + (size_t)NQKV * S_ * 2;
constexpr size_t WS_G = WS_A2 + (size_t)K2 * S_ * 2;
constexpr size_t WS_CNT = WS_G + S_ * 4;
constexpr size_t WS_PART = WS_CNT + 65536;

#define WAIT_V(n) asm volatile("s_waitcnt vmcnt(" #n ")" ::: "memory")
#define WAIT_L0 asm volatile("s_waitcnt lgkmcnt(0)" ::: "memory")
#define BAR __builtin_amdgcn_s_barrier()
#define SCHED __builtin_amdgcn_sched_barrier(0)

#define STAMP_DECL do {} while (0)
#define STAMP(kid, i) do {} while (0)
#define STAMP_END(kid, n) do {} while (0)
__device__ __forceinline__ int fresh_lane() {
  int l;
  asm volatile("v_mbcnt_lo_u32_b32 %0, -1, 0\n\tv_mbcnt_hi_u32_b32 %0, -1, %0" : "=v"(l));
  return l;
}
__device__ __forceinline__ void glds16(const void* sbase, unsigned voff, unsigned lds_addr) {
  asm volatile("s_mov_b32 m0, %2\n\ts_nop 0\n\tglobal_load_lds_dwordx4 %0, %1" ::"v"(voff), "s"(sbase), "s"(lds_addr) : "memory");
}
typedef __bf16 bf16x2_t __attribute__((ext_vector_type(2)));
typedef float f32x2 __attribute__((ext_vector_type(2)));
__device__ __forceinline__ unsigned cvtpk(float lo, float hi) {
  return __builtin_bit_cast(unsigned, __builtin_convertvector((f32x2){lo, hi}, bf16x2_t));
}
__device__ __forceinline__ bf16x8 lds_b128(const char LAS* p) { return *(const LAS bf16x8*)p; }
__device__ __forceinline__ s16x4 lds_tr(const char LAS* p) {
  return __builtin_bit_cast(s16x4, __builtin_amdgcn_ds_read_tr16_b64_v4i16((LAS s16x4*)p));
}
__device__ __forceinline__ bf16x8 cat8(s16x4 a, s16x4 b) {
  return (bf16x8){a[0], a[1], a[2], a[3], b[0], b[1], b[2], b[3]};
}
__device__ __forceinline__ f32x2 geglu2(f32x2 x, f32x2 u) {
  const float c0 = -2.0f * 0.7978845608028654f * 1.4426950408889634f;
  const float c1 = c0 * 0.044715f;
  const f32x2 t = x * x;
  const f32x2 z = x * (t * c1 + c0);
  f32x2 e; e.x = __builtin_amdgcn_exp2f(z.x); e.y = __builtin_amdgcn_exp2f(z.y);
  const f32x2 d = e + 1.0f;
  f32x2 r; r.x = __builtin_amdgcn_rcpf(d.x); r.y = __builtin_amdgcn_rcpf(d.y);
  return (x * u) * r;
}
__device__ __forceinline__ void glds4(const void* sbase, unsigned voff, unsigned lds_addr) {
  asm volatile("s_mov_b32 m0, %2\n\ts_nop 0\n\tglobal_load_lds_dword %0, %1" ::"v"(voff), "s"(sbase), "s"(lds_addr) : "memory");
}

constexpr int K2_SLOT = 24576;
constexpr int K2_G = 6 * K2_SLOT;
constexpr int K2_LDS = K2_G + 1024;
constexpr int AT_K = 3 * K2_SLOT;
constexpr int AT_V = AT_K + 32768;
constexpr int AT_V1 = AT_V + 16384 + 128;
static_assert(AT_V1 + 16384 <= K2_LDS, "lds");
#define NO_ 0
constexpr int NH = 28, NO = NO_, NK = NH + NO, NC = 16 - 2 * NO, NS = NK + NC;

__device__ __forceinline__ void st16_sc1(void* p, u32x4 w) {
  asm volatile("global_store_dwordx4 %0, %1, off sc1\n\ts_nop 1" ::"v"(p), "v"(w) : "memory");
}
template <int SC1>
__device__ __forceinline__ void glds16x4(const void* sbase, unsigned voff, unsigned lds_addr) {
  if (SC1)
    asm volatile("s_mov_b32 m0, %2\n\ts_nop 0\n\tglobal_load_lds_dwordx4 %0, %1 sc1\n\tglobal_load_lds_dwordx4 %0, %1 offset:1024 sc1\n\t"
                 "global_load_lds_dwordx4 %0, %1 offset:2048 sc1\n\tglobal_load_lds_dwordx4 %0, %1 offset:3072 sc1" ::"v"(voff), "s"(sbase), "s"(lds_addr) : "memory");
  else
    asm volatile("s_mov_b32 m0, %2\n\ts_nop 0\n\tglobal_load_lds_dwordx4 %0, %1\n\tglobal_load_lds_dwordx4 %0, %1 offset:1024\n\t"
                 "global_load_lds_dwordx4 %0, %1 offset:2048\n\tglobal_load_lds_dwordx4 %0, %1 offset:3072" ::"v"(voff), "s"(sbase), "s"(lds_addr) : "memory");
}
__device__ __forceinline__ void glds16x2(const void* sbase, unsigned voff0, unsigned voff1m, unsigned lds_addr) {
  asm volatile("s_mov_b32 m0, %3\n\ts_nop 0\n\tglobal_load_lds_dwordx4 %0, %2\n\tglobal_load_lds_dwordx4 %1, %2 offset:1024" ::"v"(voff0), "v"(voff1m),
               "s"(sbase), "s"(lds_addr) : "memory");
}
#define LP(a) ((const char LAS*)(uintptr_t)(a))

__device__ __forceinline__ float vsub(float a, float b) { float r; asm("v_sub_f32 %0, %1, %2" : "=v"(r) : "v"(a), "v"(b)); return r; }
__device__ __forceinline__ float vmul(float a, float b) { float r; asm("v_mul_f32 %0, %1, %2" : "=v"(r) : "v"(a), "v"(b)); return r; }
template <bool FIRST, class KW, class VL, class VW, class PF>
__device__ __forceinline__ void attn_wave(const char LAS* lds, char* ws, int wid, int lane, int head, int qrow, const bf16x8 (&qf)[2], const float& gval,
                                          KW kwait, VL vload, VW vwait, PF pf) {
  const int r32 = lane & 31, hi = lane >> 5;
  const int kb0 = FIRST ? 8 - wid : 0;
  const int dq = r32 - 4 * hi;
  const char LAS* kbase = lds + AT_K + wid * 1024 + r32 * 32 + ((hi ^ ((r32 >> 3) & 1)) << 4);
  const f32x16 zero = {0.f, 0.f, 0.f, 0.f, 0.f, 0.f, 0.f, 0.f, 0.f, 0.f, 0.f, 0.f, 0.f, 0.f, 0.f, 0.f};
  bf16x8 kf[9][2];
  f32x16 sb[3];
#define QKA(kb, dst, basev) dst = __builtin_amdgcn_mfma_f32_32x32x16_bf16(kf[kb][0], qf[0], (kb) == 0 ? mk0 : ((kb) == 8 ? mk8 : basev), 0, 0, 0)
#define QKB(kb, dst) dst = __builtin_amdgcn_mfma_f32_32x32x16_bf16(kf[kb][1], qf[1], dst, 0, 0, 0)
#define MX3(a, b, c) __builtin_fmaxf(__builtin_fmaxf(a, b), c)
  f32x16 mk0, mk8;
#pragma unroll
  for (int r = 0; r < 16; ++r) { const bool ab = ((r & 3) + 8 * (r >> 2)) > dq; mk0[r] = ab ? 0.0f : -INFINITY; mk8[r] = ab ? -INFINITY : 0.0f; }
  bf16x8 sel;
  { const short one = (((lane >> 4) & 1) == (lane & 1)) ? (short)0x3F80 : (short)0;
    sel = (bf16x8){one, one, one, one, one, one, one, one}; }
  const char LAS* vbase;
  { const int g = lane >> 4, q = (lane & 15) >> 2, p = lane & 3;
    vbase = lds + ((g & 1) ? AT_V1 : AT_V) + wid * 1024 + (4 * (g >> 1) + q) * 32 + p * 8; }
  u16* optr = (u16*)(ws + WS_A2) + ((size_t)(224 + head * 2) * S_ + qrow) * 16 + hi * 8;
  asm volatile("" : "+v"(mk0), "+v"(mk8), "+v"(sel), "+v"(vbase), "+v"(kbase), "+v"(optr));
  kwait();
#pragma unroll
  for (int kb = 0; kb < 9; ++kb) { kf[kb][0] = lds_b128(kbase + kb * 1024); kf[kb][1] = lds_b128(kbase + 16384 + kb * 1024); }
  vload();
  float ma = -INFINITY, mb = -INFINITY;
  SCHED;
#pragma unroll
  for (int kb = 0; kb < 11; ++kb) {
    const int pb = kb - 2;
    float a_ = ma, b_ = mb;
    if (kb < 9) { QKA(kb, sb[kb % 3], zero); SCHED; }
    if (pb >= 0) { const f32x16& t_ = sb[pb % 3]; a_ = MX3(a_, t_[0], t_[1]); b_ = MX3(b_, t_[2], t_[3]); a_ = MX3(a_, t_[4], t_[5]); b_ = MX3(b_, t_[6], t_[7]); SCHED; }
    if (kb < 9) { QKB(kb, sb[kb % 3]); SCHED; }
    if (pb >= 0) { const f32x16& t_ = sb[pb % 3]; a_ = MX3(a_, t_[8], t_[9]); b_ = MX3(b_, t_[10], t_[11]); a_ = MX3(a_, t_[12], t_[13]); b_ = MX3(b_, t_[14], t_[15]);
      if (FIRST) { ma = pb >= kb0 ? a_ : ma; mb = pb >= kb0 ? b_ : mb; } else { ma = a_; mb = b_; } }
    SCHED;
  }
  float m = __builtin_fmaxf(ma, mb);
  { auto rr = __builtin_amdgcn_permlane32_swap(__float_as_uint(m), __float_as_uint(m), false, false);
    m = __builtin_fmaxf(__uint_as_float(rr[0]), __uint_as_float(rr[1])); }
  f32x16 cm;
#pragma unroll
  for (int r = 0; r < 16; ++r) mk0[r] = vsub(mk0[r], m);
  asm volatile("s_nop 1" : "+v"(mk0));
  QKA(0, sb[0], cm); SCHED;
#pragma unroll
  for (int r = 0; r < 16; ++r) cm[r] = -m;
  asm volatile("" : "+v"(cm));
  QKB(0, sb[0]); SCHED;
#pragma unroll
  for (int r = 0; r < 16; ++r) mk8[r] = vsub(mk8[r], m);
  vwait();
  f32x16 o = zero;
  f32x4 lsum = {0.f, 0.f, 0.f, 0.f};
  s16x4 vf[2][4]; unsigned pk[8]; float ex[2][2];
#define SB2(kb) sb[(kb) & 1]
#define VF(kb) do { _Pragma("unroll") for (int i_ = 0; i_ < 4; ++i_) vf[(kb) & 1][i_] = lds_tr(vbase + (kb) * 1024 + i_ * 256); } while (0)
#define PW(st) __builtin_bit_cast(bf16x8, (u32x4){pk[4 * (st)], pk[4 * (st) + 1], pk[4 * (st) + 2], pk[4 * (st) + 3]})
#define PVM(kb, st) do { if (!FIRST || (kb) >= kb0) o = __builtin_amdgcn_mfma_f32_32x32x16_bf16(cat8(vf[(kb) & 1][2 * (st)], vf[(kb) & 1][2 * (st) + 1]), PW(st), o, 0, 0, 0); } while (0)
#define SUM(kb, st) do { if (!FIRST || (kb) >= kb0) lsum = __builtin_amdgcn_mfma_f32_16x16x32_bf16(sel, PW(st), lsum, 0, 0, 0); } while (0)
  VF(0);
#pragma unroll
  for (int kb = 0; kb < 9; ++kb) {
#pragma unroll
    for (int j = 0; j < 8; ++j) {
      ex[j & 1][0] = __builtin_amdgcn_exp2f(SB2(kb)[2 * j]); ex[j & 1][1] = __builtin_amdgcn_exp2f(SB2(kb)[2 * j + 1]);
      asm volatile("" : "+v"(ex[j & 1][0]), "+v"(ex[j & 1][1])); SCHED;
      if (kb + j > 0) { pk[(j + 7) & 7] = cvtpk(ex[(j + 1) & 1][0], ex[(j + 1) & 1][1]); asm volatile("" : "+v"(pk[(j + 7) & 7])); }
      if (j == 0 && kb + 1 < 9) QKA(kb + 1, SB2(kb + 1), cm);
      if (j == 1 && kb > 0) PVM(kb - 1, 1);
      if (j == 2 && kb > 0) SUM(kb - 1, 1);
      if (j == 3 && kb + 1 < 9) QKB(kb + 1, SB2(kb + 1));
      if (j == 3 && kb + 1 < 9) VF(kb + 1);
      if (j == 5) PVM(kb, 0);
      if (j == 6) pf(kb);
      if (j == 7) SUM(kb, 0);
      SCHED;
    }
  }
  pk[7] = cvtpk(ex[1][0], ex[1][1]);
  PVM(8, 1); SUM(8, 1);
  const float l = (lane & 16) ? lsum[1] : lsum[0];
  float sc = gval * __builtin_amdgcn_rcpf(l);
  { float o0_ = o[0] + 0.0f; asm volatile("s_nop 7\n\ts_nop 7" : "+v"(sc), "+v"(o0_)); }
#pragma unroll
  for (int dh = 0; dh < 2; ++dh) {
    const unsigned a0 = cvtpk(vmul(o[8 * dh + 0], sc), vmul(o[8 * dh + 1], sc)), a1 = cvtpk(vmul(o[8 * dh + 2], sc), vmul(o[8 * dh + 3], sc));
    const unsigned b0 = cvtpk(vmul(o[8 * dh + 4], sc), vmul(o[8 * dh + 5], sc)), b1 = cvtpk(vmul(o[8 * dh + 6], sc), vmul(o[8 * dh + 7], sc));
    auto r0 = __builtin_amdgcn_permlane32_swap(a0, b0, false, false);
    auto r1 = __builtin_amdgcn_permlane32_swap(a1, b1, false, false);
    u32x4 w = {r0[0], r1[0], r0[1], r1[1]};
    st16_sc1(optr + (size_t)dh * S_ * 16, w);
  }
#undef QKA
#undef QKB
#undef VF
#undef PW
#undef PVM
#undef SUM
#undef SB2
}

__device__ __forceinline__ void wait_count(unsigned* c, unsigned want) {
  unsigned spins = 0;
  while ((unsigned)__builtin_amdgcn_readfirstlane(__hip_atomic_load(c, __ATOMIC_RELAXED, __HIP_MEMORY_SCOPE_AGENT)) < want) {
    __builtin_amdgcn_s_sleep(4);
    if (++spins > (1u << 21)) break;
  }
  asm volatile("" ::: "memory");
}

__global__ __launch_bounds__(512, 2) void k2_fused(const u16* __restrict__ wd, const u16* __restrict__ wo, char* __restrict__ ws,
                                                   u16* __restrict__ out) {
  extern __shared__ __attribute__((aligned(16))) char lds_[];
  char LAS* lds = (char LAS*)lds_;
  const unsigned lds0 = (unsigned)(uintptr_t)lds;
  const int tid = threadIdx.x, lane = tid & 63;
  const int wid = __builtin_amdgcn_readfirstlane(tid >> 6);
  const int bid = blockIdx.x;
  const int xcd = bid & 7, idx = bid >> 3;
  const int sp = xcd >> 1, tn = idx & 7, tm = (xcd & 1) * 4 + (idx >> 3);
  const int mp = sp >> 1, np = sp & 1;
  const int tile = tm * 8 + tn;
  const int head = sp * 8 + tn, rb = tm;
  const int q0 = rb * 256;
  const char* qkv = ws + WS_QKV;
  unsigned* cntA = (unsigned*)(ws + WS_CNT);
  unsigned* cntP = (unsigned*)(ws + WS_CNT + 16 * 128);
  STAMP_DECL;
  STAMP(1, 0);

  const int qrow = q0 + wid * 32 + (lane & 31);
  const int rowlo = (rb > 0 ? q0 - 256 : 0) + wid * 32, rowhi = q0 + wid * 32;
  bf16x8 qf[2];
  float gval;
  {
    const char* qp = qkv + ((size_t)(head * 2) * S_ + q0 + wid * 32) * 32;
    const unsigned voffQ = (lane & 31) * 32 + (lane >> 5) * 16;
    qf[0] = *(const bf16x8*)(qp + voffQ);
    qf[1] = *(const bf16x8*)(qp + S_ * 32 + voffQ);
    gval = ((const float*)(ws + WS_G))[qrow];
    const unsigned voffK = (lane >> 1) * 32 + (((lane & 1) ^ ((lane >> 4) & 1)) << 4);

#pragma unroll
    for (int dh = 0; dh < 2; ++dh) {
      glds16(qkv + ((size_t)(64 + head * 2 + dh) * S_ + rowhi) * 32, voffK, lds0 + AT_K + dh * 16384 + (wid + 8) * 1024);
      glds16(qkv + ((size_t)(64 + head * 2 + dh) * S_ + rowlo) * 32, voffK, lds0 + AT_K + dh * 16384 + wid * 1024);
    }
  }

  const int ld = wid & 3;
  unsigned voffA, voffB0, voffB1m;
  auto set_voff = [&]() {
    const int l_ = fresh_lane();
    const int r = l_ >> 3, j = l_ & 7, q = r & 3;
    const int f0 = (q >> 1) & 1, f1 = f0 | 2;
    voffA = l_ * 16;
    voffB0 = r * 2048 + ((((j >> 1) ^ f0) << 1 | (j & 1)) << 4);
    voffB1m = (r + 8) * 2048 + ((((j >> 1) ^ f1) << 1 | (j & 1)) << 4) - 1024;
  };
  const char* hA = ws + WS_A2 + ((size_t)(sp * NH * 2 + (ld >> 1)) * S_ + tm * 256 + ((ld & 1) ^ mp) * 128) * 32;
  const char* hB = (const char*)wd + (size_t)(sp * NH * 32 + 16 * (ld & 1)) * 2048 + (size_t)(tn * 128 + ((ld >> 1) ^ np) * 64) * 2;
  const char* oA = ws + WS_A2 + ((size_t)(224 + sp * NO * 2 + (ld >> 1)) * S_ + tm * 256 + ((ld & 1) ^ mp) * 128) * 32;
  const char* oB = (const char*)wo + (size_t)(sp * NO * 32 + 16 * (ld & 1)) * 2048 + (size_t)(tn * 128 + ((ld >> 1) ^ np) * 64) * 2;
  const char* cA = ws + WS_A2 + ((size_t)(224 + 8 * NO + ld) * S_ + tm * 256 + mp * 128) * 32;
  const char* cB = (const char*)wo + (size_t)(128 * NO + 16 * ld) * 2048 + (size_t)(tn * 128 + np * 64) * 2;
  auto issue = [&](int i) {
    const unsigned dst = lds0 + (i % 6) * K2_SLOT;
    if (i < NH) {
      glds16x4<0>(hA + (size_t)i * (2 * S_ * 32), voffA, dst + ld * 4096);
      glds16x2(hB + (size_t)i * (32 * 2048), voffB0, voffB1m, dst + 16384 + ld * 2048);
    } else if (i < NK) {
      glds16x4<1>(oA + (size_t)(i - NH) * (2 * S_ * 32), voffA, dst + ld * 4096);
      glds16x2(oB + (size_t)(i - NH) * (32 * 2048), voffB0, voffB1m, dst + 16384 + ld * 2048);
    } else {
      glds16x4<1>(cA + (size_t)(i - NK) * (4 * S_ * 32), voffA, dst + ld * 4096);
      glds16x2(cB + (size_t)(i - NK) * (64 * 2048), voffB0, voffB1m, dst + 16384 + ld * 2048);
    }
  };

  {
    set_voff();
    const int hf = wid >> 2;
    unsigned voffBp = hf ? voffB1m + 1024 : voffB0;
    const char* pA = hA + hf * 2048;
    const unsigned dA = lds0 + ld * 4096 + hf * 2048, dB = lds0 + 16384 + ld * 2048 + hf * 1024;
    auto kwait = [&]() {
      asm volatile("" : "+v"(voffA), "+v"(voffBp));
      asm volatile("s_waitcnt vmcnt(0)" : "+v"(qf[0]), "+v"(qf[1]), "+v"(gval) :: "memory");
      BAR;
      asm volatile("" ::: "memory");
      STAMP(1, 1);
    };
    auto vload = [&]() {
#pragma unroll
      for (int dh = 0; dh < 2; ++dh) {
        glds16(qkv + ((size_t)(128 + head * 2 + dh) * S_ + rowlo) * 32, voffA, lds0 + (dh ? AT_V1 : AT_V) + wid * 1024);
        glds16(qkv + ((size_t)(128 + head * 2 + dh) * S_ + rowhi) * 32, voffA, lds0 + (dh ? AT_V1 : AT_V) + (wid + 8) * 1024);
      }
      if (lane < 32) *(LAS float*)(lds + K2_G + (wid * 32 + lane) * 4) = 1.0f - gval;
    };
    auto vwait = [&]() { WAIT_V(0); BAR; asm volatile("" ::: "memory"); };
    auto pf1 = [&](int n) {
      const int st = n / 3, j = n % 3;
      if (j < 2) glds16(pA + (size_t)st * (2 * S_ * 32) + j * 1024, voffA, dA + st * K2_SLOT + j * 1024);
      else glds16(hB + (size_t)st * (32 * 2048), voffBp, dB + st * K2_SLOT);
    };
    auto pf = [&](int kb) {
      if (kb < 6) pf1(kb);
      else { pf1(2 * kb - 6); pf1(2 * kb - 5); }
    };
    if (rb == 0) attn_wave<true>(lds, ws, wid, lane, head, qrow, qf, gval, kwait, vload, vwait, pf);
    else attn_wave<false>(lds, ws, wid, lane, head, qrow, qf, gval, kwait, vload, vwait, pf);
  }
  STAMP(1, 2);

  if (wid >= 4) {
    set_voff();
    WAIT_V(11);
    WAIT_L0; BAR;
    issue(4);
    WAIT_V(11);
    BAR;
    unsigned* ca = cntA + tm * 32;
    unsigned fl = 0;
    for (int P = 0; P < NS / 2; ++P) {
      const int a = 2 * P + 5;
      if (P == 9) asm volatile("global_load_dword %0, %1, off sc1" : "=v"(fl) : "v"(ca) : "memory");
      if (P == 11) {
        asm volatile("" : "+v"(fl));
        if ((unsigned)__builtin_amdgcn_readfirstlane(fl) < 256u) wait_count(ca, 256u);
      }
      if (a + 1 < NS) { issue(a); issue(a + 1); WAIT_V(12); }
      else if (a < NS) { issue(a); WAIT_V(6); }
      else { WAIT_V(0); }
      if (P == 0 && voffA == 0) __hip_atomic_fetch_add(ca, 1u, __ATOMIC_RELAXED, __HIP_MEMORY_SCOPE_AGENT);
      if (P + 1 < NS / 2) BAR;
    }
    STAMP(1, 3); STAMP(1, 4); STAMP(1, 5); STAMP(1, 6);
    STAMP_END(1, 7);
    return;
  }

  const int wm = wid & 1, wn = wid >> 1;
  const int fr = lane & 15, g4 = lane >> 4;
  const unsigned rAh = lds0 + (g4 >> 1) * 8192 + (wm * 64 + fr) * 32 + (g4 & 1) * 16;
  unsigned rAc = 0;
  unsigned rB0, rB1;
  { const int q = (lane & 15) >> 2, p = lane & 3;
    const int f = ((q >> 1) & 1) | ((g4 & 1) << 1);
    rB0 = lds0 + 16384 + g4 * 1024 + q * 128 + (((wn * 2) ^ f) << 5) + p * 8;
    rB1 = rB0 ^ 32u; }

  f32x4 acc[8][4];
#pragma unroll
  for (int m = 0; m < 8; ++m)
#pragma unroll
    for (int n = 0; n < 4; ++n) { acc[m][n] = (f32x4){0.f, 0.f, 0.f, 0.f}; asm volatile("" : "+v"(acc[m][n])); }
  bf16x8 A0[8], A1[8];
  s16x4 L0[4], H0[4], L1[4], H1[4];
#define RD(AF, BL, BH, k, so, COV) do { \
    if ((k) < 8) { const int n_ = (k) >> 1; const unsigned b_ = ((n_ & 1) ? rB1 : rB0) + (so); \
      if ((k) & 1) BH[n_] = lds_tr(LP(b_) + (n_ >> 1) * 4096 + 512); else BL[n_] = lds_tr(LP(b_) + (n_ >> 1) * 4096); } \
    else { const int m_ = (k) - 8; \
      if (COV) AF[m_] = lds_b128(LP(rAc + (so)) + (m_ >> 2) * 8192 + (m_ & 3) * 512); \
      else AF[m_] = lds_b128(LP(rAh + (so)) + (m_ >> 2) * 4096 + (m_ & 3) * 512); } } while (0)
#define MFH(AF, BL, BH, k) acc[(k) >> 2][(k) & 3] = __builtin_amdgcn_mfma_f32_16x16x32_bf16(cat8(BL[(k) & 3], BH[(k) & 3]), AF[(k) >> 2], acc[(k) >> 2][(k) & 3], 0, 0, 0)
#define STAGE_H(AC, LC, HC, AN, LN, HN, so, COV) do { \
    _Pragma("unroll") for (int k = 0; k < 32; ++k) { MFH(AC, LC, HC, k); if (k >= 1 && k < 17) RD(AN, LN, HN, k - 1, so, COV); SCHED; } } while (0)

  WAIT_V(11); WAIT_L0; BAR;
  asm volatile("" ::: "memory");
#pragma unroll
  for (int k = 0; k < 16; ++k) RD(A0, L0, H0, k, 0, 0);
  WAIT_V(5); WAIT_L0; BAR;
  SCHED;
  STAMP(1, 3);
  int s1 = 1;
#pragma unroll 1
  for (int ph = 0; ph < 2; ++ph) {
    const int np_ = ph ? (NO > 0 ? NO / 2 - 1 : 0) : (NO > 0 ? NH / 2 : NH / 2 - 1);
#pragma unroll 1
    for (int P = 0; P < np_; ++P) {
      const int s2 = (s1 == 5) ? 0 : s1 + 1;
      STAGE_H(A0, L0, H0, A1, L1, H1, s1 * K2_SLOT, 0);
      STAGE_H(A1, L1, H1, A0, L0, H0, s2 * K2_SLOT, 0);
      s1 = s2 + 1;
      WAIT_V(2);
      if (P == 2) { WAIT_V(0); if (fresh_lane() == 0) __hip_atomic_fetch_add(cntA + tm * 32, 1u, __ATOMIC_RELAXED, __HIP_MEMORY_SCOPE_AGENT); }
      WAIT_L0; BAR; SCHED;
    }
    if (ph == 0 && NO > 0) {
      const int l4 = fresh_lane();
#pragma unroll
      for (int m = 0; m < 8; ++m) {
        const float sc = *(const LAS float*)(lds + K2_G + (((m >> 2) ^ mp) * 128 + wm * 64 + (m & 3) * 16 + (l4 & 15)) * 4);
#pragma unroll
        for (int n = 0; n < 4; ++n) acc[m][n] *= sc;
      }
      SCHED;
    }
  }
  STAGE_H(A0, L0, H0, A1, L1, H1, ((NK - 1) % 6) * K2_SLOT, 0);
  { const int l2 = fresh_lane(); rAc = lds0 + (l2 >> 5) * 4096 + (wm * 64 + (l2 & 15)) * 32 + ((l2 >> 4) & 1) * 16; }
  STAGE_H(A1, L1, H1, A0, L0, H0, (NK % 6) * K2_SLOT, 1);
  WAIT_L0; BAR; SCHED;
  STAMP(1, 4);

  auto rs = __builtin_amdgcn_make_buffer_rsrc((void*)(ws + WS_PART), 0, 16u << 20, 0x00020000);
  const int l3 = fresh_lane();
  if (NO == 0) {
#pragma unroll
    for (int m = 0; m < 8; ++m) {
      const float sc = *(const LAS float*)(lds + K2_G + (((m >> 2) ^ mp) * 128 + wm * 64 + (m & 3) * 16 + (l3 & 15)) * 4);
#pragma unroll
      for (int n = 0; n < 4; ++n) acc[m][n] *= sc;
    }
  }
#pragma unroll
  for (int d = 3; d >= 1; --d) {
    const int h = d >> 1, c = d & 1;
#pragma unroll
    for (int mb = 0; mb < 4; ++mb) {
      const f32x4 u = acc[4 * h + mb][2 * c], v = acc[4 * h + mb][2 * c + 1];
      u32x4 w = {cvtpk(u[0], u[1]), cvtpk(u[2], u[3]), cvtpk(v[0], v[1]), cvtpk(v[2], v[3])};
      const unsigned off = (((((unsigned)tile * 4 + (d ^ sp)) * 4 + sp) * 4 + wid) * 4 + mb) * 1024u + l3 * 16;
      __builtin_amdgcn_raw_buffer_store_b128(w, rs, off, 0, 16);
    }
  }
  f32x4 acK[4][2];
#pragma unroll
  for (int mb = 0; mb < 4; ++mb)
#pragma unroll
    for (int j = 0; j < 2; ++j) acK[mb][j] = acc[mb][j];
  SCHED;
#define MFC(AF, BL, BH, k) acK[((k) >> 1) & 3][(k) & 1] = __builtin_amdgcn_mfma_f32_16x16x32_bf16(cat8(BL[2 * ((k) >> 3) + ((k) & 1)], BH[2 * ((k) >> 3) + ((k) & 1)]), \
                                                             AF[4 * ((k) >> 3) + (((k) >> 1) & 3)], acK[((k) >> 1) & 3][(k) & 1], 0, 0, 0)
#define STAGE_C(AC, LC, HC, AN, LN, HN, so) do { \
    _Pragma("unroll") for (int k = 0; k < 16; ++k) { MFC(AC, LC, HC, k); RD(AN, LN, HN, k, so, 1); SCHED; } } while (0)
  u32x4 part[3][4];
  unsigned* myc = cntP + (tile * 4 + wid) * 32;
  unsigned flagv = 0;
#define XSIG 1
#define XLD 2
#pragma unroll
  for (int P = NK / 2; P < NS / 2; ++P) {
    STAGE_C(A0, L0, H0, A1, L1, H1, ((2 * P + 1) % 6) * K2_SLOT);
    if (P + 1 < NS / 2) { STAGE_C(A1, L1, H1, A0, L0, H0, ((2 * P + 2) % 6) * K2_SLOT); }
    else {
#pragma unroll
      for (int k = 0; k < 16; ++k) MFC(A1, L1, H1, k);
    }
    if (P == NK / 2 + XSIG) { WAIT_V(0); if (l3 == 0) __hip_atomic_fetch_add(myc, 1u, __ATOMIC_RELAXED, __HIP_MEMORY_SCOPE_AGENT); }
    if (P == NS / 2 - 2 - XLD) flagv = __hip_atomic_load(myc, __ATOMIC_RELAXED, __HIP_MEMORY_SCOPE_AGENT);
    if (P + 1 < NS / 2) { WAIT_L0; BAR; SCHED; }
    if (P == NS / 2 - 1 - XLD) {
      if ((unsigned)__builtin_amdgcn_readfirstlane(flagv) < 4u) wait_count(myc, 4u);
      asm volatile("" ::: "memory");
#pragma unroll
      for (int jj = 0; jj < 3; ++jj) {
        const int src = (sp + 1 + jj) & 3;
#pragma unroll
        for (int mb = 0; mb < 4; ++mb) {
          const unsigned off = (((((unsigned)tile * 4 + sp) * 4 + src) * 4 + wid) * 4 + mb) * 1024u + l3 * 16;
          part[jj][mb] = __builtin_amdgcn_raw_buffer_load_b128(rs, off, 0, 16);
        }
      }
    }
  }
  STAMP(1, 5);
  const int lane_e = fresh_lane();
#pragma unroll
  for (int mb = 0; mb < 4; ++mb) {
    f32x4 u = acK[mb][0], v = acK[mb][1];
#pragma unroll
    for (int jj = 0; jj < 3; ++jj) {
      const u32x4 w = part[jj][mb];
      u[0] += __uint_as_float(w[0] << 16); u[1] += __uint_as_float(w[0] & 0xffff0000u);
      u[2] += __uint_as_float(w[1] << 16); u[3] += __uint_as_float(w[1] & 0xffff0000u);
      v[0] += __uint_as_float(w[2] << 16); v[1] += __uint_as_float(w[2] & 0xffff0000u);
      v[2] += __uint_as_float(w[3] << 16); v[3] += __uint_as_float(w[3] & 0xffff0000u);
    }
    auto r0 = __builtin_amdgcn_permlane16_swap(cvtpk(u[0], u[1]), cvtpk(v[0], v[1]), false, false);
    auto r1 = __builtin_amdgcn_permlane16_swap(cvtpk(u[2], u[3]), cvtpk(v[2], v[3]), false, false);
    u32x4 w = {r0[0], r1[0], r0[1], r1[1]};
    const int row = tm * 256 + mp * 128 + wm * 64 + mb * 16 + (lane_e & 15);
    const int col = tn * 128 + np * 64 + wn * 32 + ((lane_e >> 4) & 1) * 16 + (lane_e >> 5) * 8;
    *(u32x4*)(out + (size_t)row * D_ + col) = w;
  }
  STAMP(1, 6);
  STAMP_END(1, 7);
}

constexpr int G1_LDS_A = 0;
constexpr int G1_LDS_B = 65536;
constexpr int G1_LDS_B4 = 131072;
constexpr int G1_LDS_GW = 147456;
constexpr int G1_LDS_DUMMY = G1_LDS_GW + 2048;
constexpr int G1_LDS = G1_LDS_DUMMY + 2048;

#define QMODE 2
#define HMODE 2
#define TBACK 0
template <int MODE, int OFF>
__device__ __forceinline__ void store_pair16(const void* sbase, unsigned voff, unsigned x0, unsigned x1, unsigned y0, unsigned y1) {
  auto r0 = __builtin_amdgcn_permlane16_swap(x0, y0, false, false);
  auto r1 = __builtin_amdgcn_permlane16_swap(x1, y1, false, false);
  u32x4 w = {r0[0], r1[0], r0[1], r1[1]};
  if (MODE == 0) asm volatile("global_store_dwordx4 %0, %1, %2 offset:%3\n\ts_nop 1" ::"v"(voff), "v"(w), "s"(sbase), "n"(OFF) : "memory");
  else if (MODE == 1) asm volatile("global_store_dwordx4 %0, %1, %2 offset:%3 nt\n\ts_nop 1" ::"v"(voff), "v"(w), "s"(sbase), "n"(OFF) : "memory");
  else asm volatile("global_store_dwordx4 %0, %1, %2 offset:%3 sc1\n\ts_nop 1" ::"v"(voff), "v"(w), "s"(sbase), "n"(OFF) : "memory");
}

__global__ __launch_bounds__(512, 2) void k_gemm1(const u16* __restrict__ x, const u16* __restrict__ wqkv, const u16* __restrict__ wg,
                                                  const u16* __restrict__ wu, const u16* __restrict__ gate_w, char* __restrict__ ws,
                                                  const u16* __restrict__ wd, const u16* __restrict__ wo) {
  extern __shared__ __attribute__((aligned(16))) char lds_[];
  char LAS* lds = (char LAS*)lds_;
  const unsigned lds0 = (unsigned)(uintptr_t)lds;
  const int tid = threadIdx.x, lane = tid & 63;
  const int wid = __builtin_amdgcn_readfirstlane(tid >> 6);
  const int wr = wid >> 2, wc = wid & 3;
  const int bid = blockIdx.x;
  const int xcd = bid & 7, idx = bid >> 3;
  const int sp = xcd >> 1, tm = (xcd & 1) * 4 + (idx >> 3), ct = idx & 7;
  const int tn = sp * 8 + ct;

  int ptype[5], pcol[5];
  {
    auto qc = [&](int i) { return (i >> 2) * 1024 + 256 * sp + 64 * (i & 3); };
    if (ct < 6) {
      ptype[0] = 1; ptype[1] = 2; pcol[0] = pcol[1] = 896 * sp + 128 * ct;
      ptype[2] = 1; ptype[3] = 2; pcol[2] = pcol[3] = 896 * sp + 128 * ct + 64;
      ptype[4] = 0; pcol[4] = qc(ct);
    } else {
      ptype[0] = 1; ptype[1] = 2; pcol[0] = pcol[1] = 896 * sp + 768 + 64 * (ct - 6);
      ptype[2] = ptype[3] = ptype[4] = 0;
      pcol[2] = qc(6 + 3 * (ct - 6)); pcol[3] = qc(7 + 3 * (ct - 6)); pcol[4] = qc(8 + 3 * (ct - 6));
    }
  }

  const char* sA = (const char*)x + (size_t)(tm * 256 + wid * 8) * 2048;
  unsigned voffA;
  { const int r = lane >> 3, j = lane & 7; voffA = r * 2048 + ((j ^ ((4 * (wid & 1) + (r >> 1)) & 7)) << 4); }
  const unsigned ldsA = lds0 + G1_LDS_A + wid * 1024;
  const int myp = wid >> 1;
  int mt = ptype[0], mc = pcol[0];
#pragma unroll
  for (int p = 1; p < 4; ++p) if (myp == p) { mt = ptype[p]; mc = pcol[p]; }
  const unsigned strB = (mt == 0) ? 6144u : 7168u;
  const char* sB = (const char*)(mt == 0 ? wqkv : (mt == 1 ? wg : wu)) + (size_t)mc * 2 + (size_t)(16 * (wid & 1)) * strB;
  unsigned voffB0, voffB1;
  { const int r = lane >> 3, j = lane & 7, q = r & 3;
    const int f0 = (q >> 1) & 1, f1 = f0 | 2;
    voffB0 = r * strB + ((((j >> 1) ^ f0) << 1 | (j & 1)) << 4);
    voffB1 = (r + 8) * strB + ((((j >> 1) ^ f1) << 1 | (j & 1)) << 4); }
  const unsigned ldsB = lds0 + G1_LDS_B + myp * 8192 + (wid & 1) * 2048;
  constexpr unsigned strB4 = 6144u;
  const char* sB4 = (const char*)wqkv + (size_t)pcol[4] * 2 + (size_t)(8 * wid) * strB4;
  unsigned voffB4;
  { const int r = lane >> 3, j = lane & 7, q = r & 3;
    const int f = ((q >> 1) & 1) | ((wid & 1) << 1);
    voffB4 = r * strB4 + ((((j >> 1) ^ f) << 1 | (j & 1)) << 4); }
  const unsigned ldsB4 = lds0 + G1_LDS_B4 + wid * 1024;

#define KC(kt) ((unsigned)((kt) < 15 ? (kt) : 15))
#define G1_A0(kt, b) do { const char* _p = sA + KC(kt) * 128u; glds16(_p, voffA, ldsA + (b) * 32768); \
                          glds16(_p + 64 * 2048, voffA, ldsA + (b) * 32768 + 8192); } while (0)
#define G1_A1(kt, b) do { const char* _p = sA + KC(kt) * 128u; glds16(_p + 128 * 2048, voffA, ldsA + (b) * 32768 + 16384); \
                          glds16(_p + 192 * 2048, voffA, ldsA + (b) * 32768 + 24576); } while (0)
#define G1_X(kt, b) do { const char* _p = sB + KC(kt) * 64u * strB; glds16(_p, voffB0, ldsB + (b) * 32768); \
                         glds16(_p, voffB1, ldsB + (b) * 32768 + 1024); } while (0)
#define G1_Y(kt, b) do { const char* _p = sB + (KC(kt) * 64u + 32u) * strB; glds16(_p, voffB0, ldsB + (b) * 32768 + 4096); \
                         glds16(_p, voffB1, ldsB + (b) * 32768 + 4096 + 1024); } while (0)
#define G1_Z(kt, b) glds16(sB4 + KC(kt) * 64u * strB4, voffB4, ldsB4 + (b) * 8192)

  STAMP_DECL;
  STAMP(0, 0);
  glds4((const char*)gate_w + wid * 256, lane * 4, lds0 + G1_LDS_GW + wid * 256);
  G1_X(0, 0); G1_A0(0, 0); G1_A1(0, 0); G1_Z(0, 0); G1_Y(0, 0); G1_X(1, 1);
#define TOUCH 1
#define TD 2
#define TPRO 1
#define CODEPF 1
  const char* tp; unsigned voffT, tstep, tlead, tmax = 15;
  if (wid == 0) { tp = (const char*)x + (size_t)(tm * 256 + ct * 32) * 2048; voffT = (lane & 31) * 2048; tstep = 128; tlead = TD; }
  else if (wid <= 5) {
    int t_ = ptype[0], c_ = pcol[0];
#pragma unroll
    for (int p = 1; p < 5; ++p) if (wid - 1 == p) { t_ = ptype[p]; c_ = pcol[p]; }
    const unsigned st_ = (t_ == 0) ? 6144u : 7168u;
    tp = (const char*)(t_ == 0 ? wqkv : (t_ == 1 ? wg : wu)) + (size_t)c_ * 2 + (size_t)((tm & 3) * 16) * st_;
    voffT = (lane & 15) * st_; tstep = 64 * st_; tlead = TD;
  } else if (wid == 6) { tp = (const char*)wd + (size_t)bid * 28672; voffT = (lane % 14) * 128; tstep = 1792; tlead = 1; }
  else if (idx >= 16 || !CODEPF) { tp = (const char*)wo + (size_t)(xcd * 16 + (idx & 15)) * 16384; voffT = (lane & 7) * 128; tstep = 1024; tlead = 1; }
  else {
    unsigned long long pc; int d;
    asm volatile("s_getpc_b64 %0\n\ts_mov_b32 %1, _Z8k2_fusedPKtS0_PcPt@rel32@lo+4" : "=s"(pc), "=s"(d));
    const bool ok = d < -4096;
    const unsigned n4k = ok ? (unsigned)(-d) / 4096u : 1u;
    tp = ok ? (const char*)(pc + (long long)d) : (const char*)wo;
    voffT = (lane & 31) * 128; tstep = 4096; tlead = (unsigned)-8; tmax = (n4k < 7u ? n4k : 7u) - 1u;
  }
  auto touch = [&](int j) { const unsigned k_ = (unsigned)j + tlead; glds4(tp + (k_ < tmax ? k_ : tmax) * tstep, voffT, lds0 + G1_LDS_DUMMY + wid * 256); };

  const int fr = lane & 15, g4 = lane >> 4;
  const char LAS* rA0 = lds + G1_LDS_A + wr * 8192 + fr * 128 + (((0 + g4) ^ (fr >> 1)) << 4);
  const char LAS* rA1 = lds + G1_LDS_A + wr * 8192 + fr * 128 + (((4 + g4) ^ (fr >> 1)) << 4);
  const char LAS* rB; const char LAS* rB4;
  { const int q = (lane & 15) >> 2, p = lane & 3;
    const int f = ((q >> 1) & 1) | ((g4 & 1) << 1);
    const int o = g4 * 1024 + q * 128 + ((wc ^ f) << 5) + p * 8;
    rB = lds + G1_LDS_B + o;
    rB4 = lds + G1_LDS_B4 + o; }

  const char LAS* rG; const char LAS* rGW;
  { const int r = tn * 8 + wid, l = lane & 31;
    rG = lds + G1_LDS_A + r * 128 + (((l >> 2) ^ ((r >> 1) & 7)) << 4) + (l & 3) * 4;
    rGW = lds + G1_LDS_GW + l * 4; }
  float gsum = 0.f;
  f32x4 acc[2][4][5];
#pragma unroll
  for (int a = 0; a < 2; ++a)
#pragma unroll
    for (int m = 0; m < 4; ++m)
#pragma unroll
      for (int p = 0; p < 5; ++p) { acc[a][m][p] = (f32x4){0.f, 0.f, 0.f, 0.f}; asm volatile("" : "+v"(acc[a][m][p])); }
  asm volatile("" : "+v"(rA0), "+v"(rA1), "+v"(rB), "+v"(rB4), "+v"(rG), "+v"(rGW), "+v"(voffT));
  bf16x8 Af[8], Bf[5];

#define G1_LDB(b, ks) do { _Pragma("unroll") for (int p = 0; p < 4; ++p) { \
      s16x4 lo = lds_tr(rB + (b) * 32768 + p * 8192 + (ks) * 4096), hi = lds_tr(rB + (b) * 32768 + p * 8192 + (ks) * 4096 + 512); Bf[p] = cat8(lo, hi); } \
    { s16x4 lo = lds_tr(rB4 + (b) * 8192 + (ks) * 4096), hi = lds_tr(rB4 + (b) * 8192 + (ks) * 4096 + 512); Bf[4] = cat8(lo, hi); } } while (0)
#define G1_LDA(b, ks) do { _Pragma("unroll") for (int m = 0; m < 8; ++m) \
      Af[m] = lds_b128(((ks) ? rA1 : rA0) + (b) * 32768 + (m >> 2) * 16384 + (m & 3) * 2048); } while (0)
#define EARLYB 0
#define G1_MMA() do { __builtin_amdgcn_s_setprio(1); _Pragma("unroll") for (int k = 0; k < 40; ++k) { const int m = k / 5, p = k % 5; \
      if (k == 40 - EARLYB) { SCHED; __builtin_amdgcn_s_setprio(3); BAR; SCHED; } \
      acc[m >> 2][m & 3][p] = __builtin_amdgcn_mfma_f32_16x16x32_bf16(Bf[p], Af[m], acc[m >> 2][m & 3][p], 0, 0, 0); } \
      __builtin_amdgcn_s_setprio(0); if (EARLYB == 0) BAR; } while (0)
#define WV_A WAIT_V(5)
#define WV_B WAIT_V(10)
  WAIT_V(4); BAR;
  STAMP(0, 1);
  if (TOUCH) {
#pragma unroll
    for (int e = TPRO; e >= 1; --e) touch(-e);
  }
  if (wr == 1) BAR;

#define G1_TILE(t, b) do { \
    G1_LDB(b, 0); G1_LDA(b, 0); \
    G1_A0((t) + 1, (b) ^ 1); G1_A1((t) + 1, (b) ^ 1); G1_Z((t) + 1, (b) ^ 1); G1_Y((t) + 1, (b) ^ 1); \
    WV_B; WAIT_L0; BAR; G1_MMA(); SCHED; \
    G1_LDB(b, 1); G1_LDA(b, 1); \
    { const unsigned xa = *(const LAS unsigned*)(rG + (b) * 32768), wa = *(const LAS unsigned*)(rGW + (t) * 128); \
      G1_X((t) + 2, b); if (TOUCH) touch(t); \
      WAIT_L0; \
      gsum = __builtin_fmaf(__uint_as_float(xa << 16), __uint_as_float(wa << 16), gsum); \
      gsum = __builtin_fmaf(__uint_as_float(xa & 0xffff0000u), __uint_as_float(wa & 0xffff0000u), gsum); \
      asm volatile("" : "+v"(gsum)); } \
    WV_A; BAR; G1_MMA(); SCHED; \
  } while (0)

#pragma unroll 1
  for (int t = 0; t < 16; t += 2) {
    G1_TILE(t, 0);
    G1_TILE(t + 1, 1);
  }
  STAMP(0, 2);
  const int lane_e = fresh_lane();
#define lane lane_e

  {
#pragma unroll
    for (int o = 16; o >= 1; o >>= 1) gsum += __shfl_xor(gsum, o);
    if (lane == 0) ((float*)(ws + WS_G))[tm * 256 + tn * 8 + wid] = 1.0f / (1.0f + __expf(-gsum));
    if (wid == 0 && lane == 0) ((unsigned*)(ws + WS_CNT))[(16 + bid) * 32] = 0u;
    if (wid == 1 && lane == 0 && bid < 16) ((unsigned*)(ws + WS_CNT))[bid * 32] = 0u;
  }

  const unsigned voffS = (((lane >> 4) & 1) * 16 + (lane & 15)) * 32 + (lane >> 5) * 16;
  const size_t rowoff = (size_t)(tm * 256 + wr * 64) * 32 + 2048;
#define G1_STQ(p) do { const float sc = pcol[p] < D_ ? QSCALE : 1.0f; \
    const char* dst = ws + WS_QKV + (size_t)(pcol[p] / 16 + wc) * (S_ * 32) + rowoff; \
    { const f32x4 u = acc[0][0][p] * sc, v = acc[0][1][p] * sc; store_pair16<QMODE, -2048>(dst, voffS, cvtpk(u[0], u[1]), cvtpk(u[2], u[3]), cvtpk(v[0], v[1]), cvtpk(v[2], v[3])); } \
    { const f32x4 u = acc[0][2][p] * sc, v = acc[0][3][p] * sc; store_pair16<QMODE, -1024>(dst, voffS, cvtpk(u[0], u[1]), cvtpk(u[2], u[3]), cvtpk(v[0], v[1]), cvtpk(v[2], v[3])); } \
    { const f32x4 u = acc[1][0][p] * sc, v = acc[1][1][p] * sc; store_pair16<QMODE, 2048>(dst, voffS, cvtpk(u[0], u[1]), cvtpk(u[2], u[3]), cvtpk(v[0], v[1]), cvtpk(v[2], v[3])); } \
    { const f32x4 u = acc[1][2][p] * sc, v = acc[1][3][p] * sc; store_pair16<QMODE, 3072>(dst, voffS, cvtpk(u[0], u[1]), cvtpk(u[2], u[3]), cvtpk(v[0], v[1]), cvtpk(v[2], v[3])); } } while (0)
#define G1_H1(pp, a, m, OFF) do { const f32x4 g0 = acc[a][m][2 * (pp)], u0 = acc[a][m][2 * (pp) + 1], g1 = acc[a][(m) + 1][2 * (pp)], u1 = acc[a][(m) + 1][2 * (pp) + 1]; \
    const f32x2 h0 = geglu2(g0.xy, u0.xy), h1 = geglu2(g0.zw, u0.zw), h2 = geglu2(g1.xy, u1.xy), h3 = geglu2(g1.zw, u1.zw); \
    store_pair16<HMODE, OFF>(dst, voffS, cvtpk(h0.x, h0.y), cvtpk(h1.x, h1.y), cvtpk(h2.x, h2.y), cvtpk(h3.x, h3.y)); } while (0)
#define G1_STH(pp) do { const char* dst = ws + WS_A2 + (size_t)(pcol[2 * (pp)] / 16 + wc) * (S_ * 32) + rowoff; \
    G1_H1(pp, 0, 0, -2048); G1_H1(pp, 0, 2, -1024); G1_H1(pp, 1, 0, 2048); G1_H1(pp, 1, 2, 3072); } while (0)
#define G1_TB(p) glds4(ws + WS_QKV + (size_t)(pcol[p] / 16 + wc) * (S_ * 32) + rowoff - 2048, voffTB, lds0 + G1_LDS_DUMMY + wid * 256)
  const unsigned voffTB = ((lane >> 4) & 1) * 4096 + (lane & 15) * 128;
  G1_STQ(4);
  if (wr == 0) BAR;
  WAIT_V(4);
  if (ptype[2] == 0) {
    G1_STQ(2); G1_STQ(3);
    G1_STH(0);
    if (TBACK) { WAIT_V(4); G1_TB(4); G1_TB(2); G1_TB(3); }
  } else {
    G1_STH(0);
    if (TBACK) { WAIT_V(4); G1_TB(4); }
    G1_STH(1);
  }
#undef lane
  STAMP(0, 3);
  STAMP(0, 4);
  STAMP_END(0, 5);
}

extern "C" void kernel_launch(void* const* d_in, const int* in_sizes, int n_in, void* d_out, int out_size, void* d_ws, size_t ws_size,
                              hipStream_t stream) {
  const u16* x = (const u16*)d_in[0];
  const u16* wqkv = (const u16*)d_in[1];
  const u16* wo = (const u16*)d_in[2];
  const u16* wg = (const u16*)d_in[4];
  const u16* wu = (const u16*)d_in[5];
  const u16* wd = (const u16*)d_in[6];
  const u16* gate_w = (const u16*)d_in[7];
  char* ws = (char*)d_ws;
  (void)hipFuncSetAttribute((const void*)k_gemm1, hipFuncAttributeMaxDynamicSharedMemorySize, G1_LDS);
  (void)hipFuncSetAttribute((const void*)k2_fused, hipFuncAttributeMaxDynamicSharedMemorySize, K2_LDS);
  k_gemm1<<<256, 512, G1_LDS, stream>>>(x, wqkv, wg, wu, gate_w, ws, wd, wo);
  k2_fused<<<256, 512, K2_LDS, stream>>>(wd, wo, ws, (u16*)d_out);
}
```
